```python
import torch

def _softmax(x, axis=-1):
    return torch.softmax(x, dim=axis)

def _gelu(x):
    return torch.nn.functional.gelu(x, approximate='tanh')

def _silu(x):
    return torch.nn.functional.silu(x)

def _sigmoid(x):
    return torch.sigmoid(x)

def _layernorm(x, axis=-1, eps=1e-05):
    m = x.mean(dim=axis, keepdim=True)
    v = x.var(dim=axis, keepdim=True, unbiased=False)
    return (x - m) / torch.sqrt(v + eps)

def _scatter_add(tgt, idx, src, axis=0):
    return tgt.clone().index_add_(axis, idx, src)

def _segment_sum(src, seg, n):
    out = torch.zeros((n,) + src.shape[1:], dtype=src.dtype)
    return out.index_add_(0, seg, src)

def _pad(x, pw):
    flat = tuple((v for pair in reversed(pw) for v in pair))
    return torch.nn.functional.pad(x, flat)
kernel_name = 'standalone__ragged_sequences_varlen_flash_attn__ragged'

def setup_inputs():

    _w = lambda a: (lambda t: t.to(torch.bfloat16) if t.is_floating_point() else t)(torch.as_tensor(a))
    cu = torch.tensor([0, 899, 1287, 3063, 3381, 3962, 4737, 5351, 7180], dtype=torch.int64)
    T = int(cu[-1])
    Hq, Hkv, D = (16, 4, 128)
    assert Hq % Hkv == 0
    return {'q': _w((lambda *s: torch.randn(*s, dtype=torch.bfloat16) * 0.8)(T, Hq, D)), 'k': _w((lambda *s: torch.randn(*s, dtype=torch.bfloat16) * 0.8)(T, Hkv, D)), 'v': _w((lambda *s: torch.randn(*s, dtype=torch.bfloat16) * 0.05)(T, Hkv, D)), 'cu': _w(cu)}

def reference(q, k, v, cu):
    T, Hq, D = q.shape
    Hkv = k.shape[1]
    rep = Hq // Hkv
    scale = D ** (-0.5)
    outs = []
    for i in range(len(cu) - 1):
        s, e = (int(cu[i]), int(cu[i + 1]))
        L = e - s
        qi = q[s:e].clone()
        ki = (lambda x, n, axis: torch.repeat_interleave(x, n, dim=axis))(k[s:e], rep, axis=1)
        vi = (lambda x, n, axis: torch.repeat_interleave(x, n, dim=axis))(v[s:e], rep, axis=1)
        logits = torch.einsum('qhd,khd->hqk', qi, ki) * scale
        ar = torch.arange(L)
        neg = (lambda x, dt: x.to(dt))(ar[None, :] > ar[:, None], logits.dtype) * -1000000000.0
        p = _softmax(logits + neg[None, :, :], axis=-1)
        outs.append(torch.einsum('hqk,khd->qhd', p, vi))
    return torch.concatenate(outs, axis=0)
```

```cpp
#include <hip/hip_runtime.h>
#include <hip/hip_bf16.h>
#include <stdint.h>

typedef unsigned u32x4 __attribute__((ext_vector_type(4)));
constexpr int HQ = 16, HKV = 4, D = 128;

__device__ __forceinline__ float bf_lo(unsigned w) { return __uint_as_float(w << 16); }
__device__ __forceinline__ float bf_hi(unsigned w) { return __uint_as_float(w & 0xffff0000u); }
__device__ __forceinline__ unsigned pack_bf16(float a, float b) {
    typedef float f32x2 __attribute__((ext_vector_type(2)));
    typedef __bf16 bf16x2 __attribute__((ext_vector_type(2)));
    f32x2 v = {a, b}; bf16x2 r = __builtin_convertvector(v, bf16x2); return __builtin_bit_cast(unsigned, r);
}

__device__ void slow_rows(const unsigned short* q, const unsigned short* k, const unsigned short* v, unsigned short* out,
                          int s0, int L, int g, int p0) {
    const int tid = threadIdx.x, row = tid >> 1, half = tid & 1;
    const int pos = p0 + (row >> 2), h = g * 4 + (row & 3);
    const bool valid = pos < L;
    const int posc = valid ? pos : L - 1;
    float qf[64], o[64];
    const u32x4* qp = (const u32x4*)(q + ((size_t)(s0 + posc) * HQ + h) * D + half * 64);
    const float c = 0.08838834764831845f * 1.4426950408889634f;
#pragma unroll
    for (int i = 0; i < 8; ++i) { u32x4 w = qp[i];
#pragma unroll
        for (int e = 0; e < 4; ++e) { qf[i * 8 + 2 * e] = bf_lo(w[e]) * c; qf[i * 8 + 2 * e + 1] = bf_hi(w[e]) * c; } }
#pragma unroll
    for (int i = 0; i < 64; ++i) o[i] = 0.f;
    float m = -3.0e38f, l = 0.f;
    int kend = p0 + 32; if (kend > L) kend = L;
    for (int j = 0; j < kend; ++j) {
        const u32x4* kp = (const u32x4*)(k + ((size_t)(s0 + j) * HKV + g) * D + half * 64);
        const u32x4* vp = (const u32x4*)(v + ((size_t)(s0 + j) * HKV + g) * D + half * 64);
        float s = 0.f;
#pragma unroll
        for (int i = 0; i < 8; ++i) { u32x4 w = kp[i];
#pragma unroll
            for (int e = 0; e < 4; ++e) { s = fmaf(qf[i * 8 + 2 * e], bf_lo(w[e]), s); s = fmaf(qf[i * 8 + 2 * e + 1], bf_hi(w[e]), s); } }
        s += __shfl_xor(s, 1);
        if (j <= posc) {
            const float mn = fmaxf(m, s), a = exp2f(m - mn), p = exp2f(s - mn);
            l = l * a + p; m = mn;
#pragma unroll
            for (int i = 0; i < 8; ++i) { u32x4 w = vp[i];
#pragma unroll
                for (int e = 0; e < 4; ++e) { o[i * 8 + 2 * e] = fmaf(o[i * 8 + 2 * e], a, p * bf_lo(w[e])); o[i * 8 + 2 * e + 1] = fmaf(o[i * 8 + 2 * e + 1], a, p * bf_hi(w[e])); } }
        }
    }
    if (valid) {
        const float il = 1.f / l;
        u32x4* op = (u32x4*)(out + ((size_t)(s0 + pos) * HQ + h) * D + half * 64);
#pragma unroll
        for (int i = 0; i < 8; ++i) { u32x4 w;
#pragma unroll
            for (int e = 0; e < 4; ++e) w[e] = pack_bf16(o[i * 8 + 2 * e] * il, o[i * 8 + 2 * e + 1] * il);
            op[i] = w; }
    }
}

__global__ __launch_bounds__(256, 1) void slow_kernel(const unsigned short* q, const unsigned short* k, const unsigned short* v,
                                                      const int* cu, int nseq, unsigned short* out) {
    int item = blockIdx.x;
    int base = 0;
    for (int s = 0; s < nseq; ++s) {
        const int s0 = cu[s], L = cu[s + 1] - s0;
        const int n = (L + 31) / 32 * HKV;
        while (item < base + n) {
            const int r = item - base;
            slow_rows(q, k, v, out, s0, L, r & 3, (r >> 2) * 32);
            item += gridDim.x;
        }
        base += n;
    }
}

extern "C" void kernel_launch(void* const* d_in, const int* in_sizes, int n_in, void* d_out, int out_size, void* d_ws, size_t ws_size,
                              hipStream_t stream) {
    const int nseq = in_sizes[3] - 1;
    slow_kernel<<<1024, 256, 0, stream>>>((const unsigned short*)d_in[0], (const unsigned short*)d_in[1], (const unsigned short*)d_in[2],
                                         (const int*)d_in[3], nseq, (unsigned short*)d_out);
}
```

```cpp
#include <hip/hip_runtime.h>
#include <stdint.h>

typedef unsigned u32x4 __attribute__((ext_vector_type(4)));
typedef unsigned short u16;
constexpr int HQ = 16, HKV = 4, D = 128;
#define TSTART __builtin_amdgcn_s_memrealtime
#define VOX i * 4096 + (gq & 1) * 32 + (gq >> 1) * 16
#define STR2(x) #x
#define STR(x) STR2(x)
#define PFN 2
#define THR_BITS 0x4e800000u
#define CD_A 64
#define CD_B 102
#define CS_A 19
#define CS_B 58
#define OFF_D 0
#define ODDB 44
#define KSH 3
#define SLK 40
#define TLX 20
#define TL0 4
#define TLS 3
#define SLK2 40
#define PEN 100
#define PEN_T 260
#define THR_F 65

__device__ __forceinline__ float bf_lo(unsigned w) { return __uint_as_float(w << 16); }
__device__ __forceinline__ float bf_hi(unsigned w) { return __uint_as_float(w & 0xffff0000u); }
__device__ __forceinline__ unsigned pack_bf16(float a, float b) {
    typedef float f32x2 __attribute__((ext_vector_type(2)));
    typedef __bf16 bf16x2 __attribute__((ext_vector_type(2)));
    f32x2 v = {a, b}; bf16x2 r = __builtin_convertvector(v, bf16x2); return __builtin_bit_cast(unsigned, r);
}

__device__ __forceinline__ void slow_rows(const u16* q, const u16* k, const u16* v, u16* out, int s0, int L, int h, int p0) {
    const int lane = threadIdx.x & 63, row = lane >> 1, half = lane & 1, g = h >> 2;
    const int pos = p0 + row;
    const bool valid = pos < L;
    const int posc = valid ? pos : L - 1;
    float qf[64], o[64];
    const u32x4* qp = (const u32x4*)(q + ((size_t)(s0 + posc) * HQ + h) * D + half * 64);
    const float c = 0.08838834764831845f * 1.4426950408889634f;
#pragma unroll
    for (int i = 0; i < 8; ++i) { u32x4 w = qp[i];
#pragma unroll
        for (int e = 0; e < 4; ++e) { qf[i * 8 + 2 * e] = bf_lo(w[e]) * c; qf[i * 8 + 2 * e + 1] = bf_hi(w[e]) * c; } }
#pragma unroll
    for (int i = 0; i < 64; ++i) o[i] = 0.f;
    float m = -3.0e38f, l = 0.f;
    int kend = p0 + 32; if (kend > L) kend = L;
    for (int j = 0; j < kend; ++j) {
        const u32x4* kp = (const u32x4*)(k + ((size_t)(s0 + j) * HKV + g) * D + half * 64);
        const u32x4* vp = (const u32x4*)(v + ((size_t)(s0 + j) * HKV + g) * D + half * 64);
        float s = 0.f;
#pragma unroll
        for (int i = 0; i < 8; ++i) { u32x4 w = kp[i];
#pragma unroll
            for (int e = 0; e < 4; ++e) { s = fmaf(qf[i * 8 + 2 * e], bf_lo(w[e]), s); s = fmaf(qf[i * 8 + 2 * e + 1], bf_hi(w[e]), s); } }
        s += __shfl_xor(s, 1);
        if (j <= posc) {
            const float mn = fmaxf(m, s), a = exp2f(m - mn), p = exp2f(s - mn);
            l = l * a + p; m = mn;
#pragma unroll
            for (int i = 0; i < 8; ++i) { u32x4 w = vp[i];
#pragma unroll
                for (int e = 0; e < 4; ++e) { o[i * 8 + 2 * e] = fmaf(o[i * 8 + 2 * e], a, p * bf_lo(w[e])); o[i * 8 + 2 * e + 1] = fmaf(o[i * 8 + 2 * e + 1], a, p * bf_hi(w[e])); } }
        }
    }
    if (valid) {
        const float il = 1.f / l;
        u32x4* op = (u32x4*)(out + ((size_t)(s0 + pos) * HQ + h) * D + half * 64);
#pragma unroll
        for (int i = 0; i < 8; ++i) { u32x4 w;
#pragma unroll
            for (int e = 0; e < 4; ++e) w[e] = pack_bf16(o[i * 8 + 2 * e] * il, o[i * 8 + 2 * e + 1] * il);
            op[i] = w; }
    }
}

__global__ __launch_bounds__(256, 1) void slow_kernel(const u16* q, const u16* k, const u16* v, const int* cu, int nseq, u16* out) {
    int item = blockIdx.x, base = 0;
    for (int s = 0; s < nseq; ++s) {
        const int s0 = cu[s], L = cu[s + 1] - s0;
        const int n = (L + 31) / 32 * HKV;
        while (item < base + n) {
            const int r = item - base;
            slow_rows(q, k, v, out, s0, L, (r & 3) * 4 + (threadIdx.x >> 6), (r >> 2) * 32);
            item += gridDim.x;
        }
        base += n;
    }
}

__device__ __forceinline__ int rl(int x, int i) { return __builtin_amdgcn_readlane(x, i); }
#define DPP(x, old, ctrl, rm) __builtin_amdgcn_update_dpp(old, x, ctrl, rm, 0xf, false)
__device__ __forceinline__ int wscan(int x) {
    x += DPP(x, 0, 0x111, 0xf); x += DPP(x, 0, 0x112, 0xf); x += DPP(x, 0, 0x114, 0xf); x += DPP(x, 0, 0x118, 0xf);
    x += DPP(x, 0, 0x142, 0xa); x += DPP(x, 0, 0x143, 0xc); return x; }
__device__ __forceinline__ int imin(int a, int b) { return a < b ? a : b; }
__device__ __forceinline__ int wmin(int x) {
    const int M = 0x7fffffff;
    x = imin(x, DPP(x, M, 0x111, 0xf)); x = imin(x, DPP(x, M, 0x112, 0xf)); x = imin(x, DPP(x, M, 0x114, 0xf)); x = imin(x, DPP(x, M, 0x118, 0xf));
    x = imin(x, DPP(x, M, 0x142, 0xa)); x = imin(x, DPP(x, M, 0x143, 0xc)); return rl(x, 63); }
__device__ __forceinline__ int wor(int x) {
    x |= DPP(x, 0, 0x111, 0xf); x |= DPP(x, 0, 0x112, 0xf); x |= DPP(x, 0, 0x114, 0xf); x |= DPP(x, 0, 0x118, 0xf);
    x |= DPP(x, 0, 0x142, 0xa); x |= DPP(x, 0, 0x143, 0xc); return rl(x, 63); }
__device__ __forceinline__ int item_cost(int d) { const int ix = (d >> 1) & 0x7fff; return (d & 1) ? CD_A * ix + CD_B : CS_A * ix + CS_B; }

__global__ __launch_bounds__(256, 1) void attn_kernel(const u16* q, const u16* k, const u16* v, const int* cu, int nseq, int T, u16* out
    ) {
    extern __shared__ char lds[];
    const int tid = threadIdx.x, lane = tid & 63;
    const int w = __builtin_amdgcn_readfirstlane(tid >> 6);
    const int g = (blockIdx.x & 7) >> 1, bin = (blockIdx.x >> 3) * 2 + (blockIdx.x & 1);
    typedef int i32x2 __attribute__((ext_vector_type(2)));
    i32x2 cc;
    {
        const int pr = (int)(((long long)(bin * 4 + w) * T) >> 8);
        asm volatile(
          "global_load_dwordx2 %[cc], %[ca], %[cu]\n\t"
          "s_getpc_b64 s[36:37]\n\t" "s_mov_b32 s38, 20480\n\t" "s_mov_b32 s39, 0x20000\n\t" "s_and_b32 s37, s37, 0xffff\n\t"
          "s_mov_b32 m0, %[st]\n\t" "s_mov_b32 s42, %[nr]\n\t" "s_mov_b32 s43, 0x20000\n\t" "s_mov_b32 s46, %[nr]\n\t" "s_mov_b32 s47, 0x20000\n\t"
          "buffer_load_dword %[co], s[36:39], 0 offen lds\n\t"
          "s_mov_b32 s40, %[klo]\n\t" "s_mov_b32 s41, %[khi]\n\t" "s_mov_b32 s44, %[vlo]\n\t" "s_mov_b32 s45, %[vhi]\n\t" "s_mov_b32 s48, %[pr]\n\t"
          ".rept " STR(PFN) "\n\t"
          "buffer_load_dwordx4 %[po], s[40:43], s48 offen lds\n\t" "buffer_load_dwordx4 %[po], s[44:47], s48 offen lds\n\t" "s_add_u32 s48, s48, 4096\n\t"
          ".endr\n\t"
          : [cc] "=&v"(cc)
          : [ca] "v"((lane < nseq ? lane : 0) * 4), [cu] "s"(cu), [st] "s"(98304 + w * 16384), [nr] "s"(T * 1024), [co] "v"(tid * 128), [po] "v"((lane >> 4) * 1024 + (lane & 15) * 16 + g * 256),
            [klo] "s"((unsigned)(size_t)k), [khi] "s"((unsigned)((size_t)k >> 32)), [vlo] "s"((unsigned)(size_t)v), [vhi] "s"((unsigned)((size_t)v >> 32)), [pr] "s"(pr * 1024)
          : "s36", "s37", "s38", "s39", "s40", "s41", "s42", "s43", "s44", "s45", "s46", "s47", "s48", "scc", "memory");
    }
    const int gq = lane >> 4, i = lane & 15;
    int vk0 = (8 * w + ((lane >> 2) & 3)) * 1024 + gq * 64 + ((lane & 3) ^ (2 * (w & 1))) * 16, vk1 = vk0 + 3072;
    int vv0 = 2048 + (8 * w + ((lane >> 2) & 7)) * 1024 + (lane >> 5) * 64 + (((lane >> 1) ^ (lane >> 4)) & 1) * 32 + (lane & 1) * 16, vv1 = vv0 + 128 - 1024;
    int kb = (i >> 3) * 4096 + ((i >> 2) & 1) * 1024 + (i & 3) * 64 + (gq ^ (2 * (i >> 3))) * 16;
    int vb0 = (gq >> 1) * 4096 + 2048 + (4 * (gq & 1) + (i >> 2)) * 64 + (gq & 1) * 32 + (i & 3) * 8, vb1 = vb0 ^ 32;
    int vqa = ((lane >> 2) & 3) * 4096 + gq * 64 + (lane & 3) * 16, vqb = vqa ^ 32;
    int qb = 98304 + w * 16384 + (i >> 2) * 1024 + (i & 3) * 64 + (gq ^ (2 * (i >> 3))) * 16;
    int wa = 98304 + w * 16384 + i * 256 + (((gq >> 1) ^ i) * 16) + (gq & 1) * 8, ra = 98304 + w * 16384 + gq * 256 + (i ^ gq) * 16;
    int vo = gq * 4096 + i * 16, md = i - 4 * gq;
    const size_t kp = (size_t)k + g * 256, vp = (size_t)v + g * 256 - 4096, qp = (size_t)q + (4 * g + w) * 256, op = (size_t)out + (4 * g + w) * 256;
    asm volatile("s_waitcnt vmcnt(1 + 2 * " STR(PFN) ")" : "+v"(cc), "+v"(vk0), "+v"(vk1), "+v"(vv0), "+v"(vv1), "+v"(kb), "+v"(vb0), "+v"(vb1), "+v"(vqa), "+v"(vqb), "+v"(qb), "+v"(wa), "+v"(ra), "+v"(vo), "+v"(md) :: "memory");
    const int s0l = lane < nseq ? cc.x : 0, Ll = lane < nseq ? cc.y - cc.x : 0;
    const int nsl = (Ll + 31) >> 5, ndl = nsl >> 1;
    const int tot = rl(wscan((CD_A / 2) * ndl * (ndl - 1) + CD_B * ndl + ((nsl & 1) ? CS_A * (nsl - 1) + CS_B : 0)), 63);
    const int thr = (int)(((unsigned)tot * THR_F) >> 12);
    const int jmax = thr < CD_B ? 0 : (thr - CD_B) / CD_A + 1;
    const int jdl = ndl < jmax ? ndl : jmax, cntl = nsl - jdl;
    const int incl = wscan(cntl), N = rl(incl, 63);
    const int pk1 = (incl - cntl) | (jdl << 16), pk2 = jdl | (nsl << 16);
    int* sorted = (int*)(lds + 5 * 16384);
    for (int t0 = w * 64; t0 < N; t0 += 256) {
        const int t = t0 + lane;
        int sq = 0;
        for (int s4 = 0; s4 < nseq - 1; s4 += 4)
#pragma unroll
            for (int s = s4; s < s4 + 4; ++s) sq += t >= rl(incl, s);
        const int p1 = __builtin_amdgcn_ds_bpermute(sq << 2, pk1), r = t - (p1 & 0xffff), jm = p1 >> 16;
        const bool du = r < jm;
        const int ix = du ? r : r + jm;
        const int xo = du ? CD_A * ix + CD_B - CS_B : CS_A * ix + CS_B - CD_B;
        const int fo = du ? (xo <= 0 ? 0 : (xo + CS_A - 1) / CS_A) : (xo < 0 ? 0 : xo / CD_A + 1);
        const unsigned fdb = du ? ix : fo, fsb = du ? fo : ix; const int sqd = du ? sq : 99999, sqs = du ? 99999 : sq;
        unsigned rank = 0;
        for (int s4 = 0; s4 < nseq; s4 += 4)
#pragma unroll
        for (int s = s4; s < s4 + 4; ++s) {
            const unsigned p2 = rl(pk2, s), jd = p2 & 0xffff, ns = p2 >> 16;
            const unsigned fd = fdb + (s >= sqd), fs = fsb + (s >= sqs);
            rank += __builtin_elementwise_sub_sat(jd, fd) + __builtin_elementwise_sub_sat(ns, fs < 2 * jd ? 2 * jd : fs);
        }
        if (t < N) sorted[rank] = (sq << 16) | (ix << 1) | (int)du;
    }
    asm volatile("s_waitcnt lgkmcnt(0)\n\ts_barrier" ::: "memory");
    int d = lane < N ? sorted[lane] : -1;
    int early;
    {
        const int d0 = rl(d, bin), s = d0 < 0 ? 0 : d0 >> 16, ix = (d0 >> 1) & 0x7fff, it = (d0 & 1) ? 2 * ix + 1 : ix;
        const int s0 = rl(s0l, s), L = rl(Ll, s);
        early = d0 >= 0 && it >= 1 - 1;
        if (early) {
            asm volatile(
      "s_mov_b32 s36, %[klo]\n\t"
      "s_mov_b32 s37, %[khi]\n\t"
      "s_mov_b32 s39, 0x20000\n\t"
      "s_mov_b32 s40, %[vlo]\n\t"
      "s_mov_b32 s41, %[vhi]\n\t"
      "s_mov_b32 s43, 0x20000\n\t"
      "s_mov_b32 s44, %[qlo]\n\t"
      "s_mov_b32 s45, %[qhi]\n\t"
      "s_mov_b32 s47, 0x20000\n\t"
      "s_mov_b32 s54, %[woff]\n\t"
      "s_lshl_b32 s83, %[woff], 2\n\t"
      "s_add_u32 s83, s83, 98304\n\t"
      "v_mov_b32 v43, 0x80000000\n\t"
      "s_mov_b32 s55, %[fa]\n\t"
      "s_mov_b32 s38, %[fb]\n\t"
      "s_add_u32 s42, s38, 4096\n\t"
      "s_lshl_b32 s65, %[fa], 2\n\t"
      "s_lshl_b32 s66, %[fb], 2\n\t"
      "s_add_u32 m0, s54, 0\n\t"
      "s_nop 0\n\t"
      "buffer_load_dwordx4 %[vk0], s[36:39], s55 offen lds\n\t"
      "buffer_load_dwordx4 %[vk1], s[36:39], s55 offen offset:1024 lds\n\t"
      "s_mov_b32 s46, s66\n\t"
      "s_mov_b32 s69, s65\n\t"
      "s_add_u32 m0, s83, 8192\n\t"
      "s_nop 0\n\t"
      "buffer_load_dwordx4 %[vqa], s[44:47], s69 offen offset:0  lds\n\t"
      "s_add_u32 s69, s69, 15360\n\t"
      "buffer_load_dwordx4 %[vqa], s[44:47], s69 offen offset:1024  lds\n\t"
      "s_add_u32 s69, s69, 15360\n\t"
      "buffer_load_dwordx4 %[vqb], s[44:47], s69 offen offset:2048  lds\n\t"
      "s_add_u32 s69, s69, 15360\n\t"
      "buffer_load_dwordx4 %[vqb], s[44:47], s69 offen offset:3072  lds\n\t"
      "s_add_u32 s69, s69, 19456\n\t"
      "s_add_u32 m0, s83, 12288\n\t"
      "s_nop 0\n\t"
      "buffer_load_dwordx4 %[vqa], s[44:47], s69 offen offset:0  lds\n\t"
      "s_add_u32 s69, s69, 15360\n\t"
      "buffer_load_dwordx4 %[vqa], s[44:47], s69 offen offset:1024  lds\n\t"
      "s_add_u32 s69, s69, 15360\n\t"
      "buffer_load_dwordx4 %[vqb], s[44:47], s69 offen offset:2048  lds\n\t"
      "s_add_u32 s69, s69, 15360\n\t"
      "buffer_load_dwordx4 %[vqb], s[44:47], s69 offen offset:3072  lds\n\t"
      "s_add_u32 s69, s69, 19456\n\t"
      "s_add_u32 m0, s54, 0\n\t"
      "s_nop 0\n\t"
      "buffer_load_dwordx4 %[vv0], s[40:43], s55 offen offset:2048 lds\n\t"
      "buffer_load_dwordx4 %[vv1], s[40:43], s55 offen offset:3072 lds\n\t"
              :
              : [fa] "s"((s0 + 32 * it) * 1024), [fb] "s"((s0 + L) * 1024), [fc] "s"(d0 & 1), [fa1] "s"((d0 & 1) ? (s0 + 32 * it - 32) * 1024 : s0 * 1024), [fa2] "s"((d0 & 1) ? s0 * 1024 : (s0 + 32) * 1024), [vk0] "v"(vk0), [vk1] "v"(vk1), [vv0] "v"(vv0), [vv1] "v"(vv1), [vqa] "v"(vqa), [vqb] "v"(vqb),
                [klo] "s"((unsigned)kp), [khi] "s"((unsigned)(kp >> 32)), [vlo] "s"((unsigned)vp), [vhi] "s"((unsigned)(vp >> 32)),
                [qlo] "s"((unsigned)qp), [qhi] "s"((unsigned)(qp >> 32)), [woff] "s"(w * 4096)
              : "v43","s36","s37","s38","s39","s40","s41","s42","s43","s44","s45","s46","s47","s48","s49","s50","s51","s52","s53","s54","s55","s56","s57","s58","s59","s60","s61","s62","s63","s64","s65","s66","s67","s68","s69","s70","s71","s72","s73","s74","s75","s76","s77","s78","s79","s80","s81","s82","s83","s84","s85","s86","s87","s88","s89","s90","s91","s92","s93","s94","s95","scc","memory");
        }
    }
    int load = d < 0 ? 0 : item_cost(d) + (d & 1) * OFF_D + (lane & 1) * ODDB, next = N < 64 ? N : 64, cnt = bin < N, mine = lane == 0 ? rl(d, bin) : -1, cb = 1;
    int rank, key = 0;
    int NE = N, cutp = 0x7fffffff, dt = -1;
    if (N > 96 && N <= 160) {
        const int d1 = sorted[64 + lane < N ? 64 + lane : 0], d2 = sorted[128 + lane < N ? 128 + lane : 0], dl = sorted[N - 1];
        const int avg = rl(wscan(load + (64 + lane < N ? item_cost(d1) : 0) + (128 + lane < N ? item_cost(d2) + TLX : 0)), 63) >> 6;
        const int z = imin(32, __builtin_popcountll(__builtin_amdgcn_ballot_w64(load + item_cost(dl) > avg + SLK2))), P = N - 128 + z;
        if (P > 0) {
            const int a = imin(P, __builtin_popcountll(__builtin_amdgcn_ballot_w64(load + item_cost(rl(d1, 63 - z)) > avg + SLK)) - z);
            const int e = (((63 - lane) >> 1) - TL0) & 31, tl = (e < 8 * TLS ? e % TLS * 8 + e / TLS : e) | (lane & 1) << 5;
            NE = 128 - z; cutp = NE - (a > 0 ? a : 0);
            if (tl < P) { dt = sorted[cutp + tl]; load += item_cost(dt) + TLX; }
        }
    }
    while (next < NE) {
        if (OFF_D == 0 && ODDB == 0 && TLX < 0 && next == 64) rank = 63 - lane;
        else {
        unsigned eq0 = ~0u, eq1 = ~0u; rank = 0;
        const int lo = wmin(load);
        key = imin((load - lo) >> KSH, 127); load = key < 127 ? lo + (key << KSH) : load;
#pragma unroll
        for (int bit = 64; bit; bit >>= 1) {
            const bool b = key & bit;
            const unsigned long long B = __builtin_amdgcn_ballot_w64(b);
            const unsigned n0 = eq0 & ~(unsigned)B, n1 = eq1 & ~(unsigned)(B >> 32);
            const int pc = __builtin_popcount(n0) + __builtin_popcount(n1);
            rank += b ? pc : 0;
            eq0 = b ? eq0 ^ n0 : n0; eq1 = b ? eq1 ^ n1 : n1;
        }
        rank += __builtin_amdgcn_mbcnt_hi(eq1, __builtin_amdgcn_mbcnt_lo(eq0, 0));
        }
        const bool has = next + rank < NE && key < 127;
        d = sorted[has ? next + rank + (next + rank >= cutp ? N - NE : 0) : 0];
        const int ic = item_cost(d), nl = has ? load + ic + (ic < PEN_T ? PEN : 0) : 0x7fffffff;
        const int mn = wmin(nl);
        const bool acc = has && load <= mn;
        const unsigned long long A = __builtin_amdgcn_ballot_w64(acc);
        if (acc) { ++cb; load = cb >= 58 ? 0x3fffffff : nl; }
        if ((A >> bin) & 1) { const int dm = rl(d, bin); if (lane == cnt) mine = dm; ++cnt; }
        next += __builtin_popcountll(A);
    }
    { const int dm = rl(dt, bin); if (dm >= 0) { if (lane == cnt) mine = dm; ++cnt; } }
    const int ms = mine < 0 ? 0 : mine >> 16;
    const int s0m = __builtin_amdgcn_ds_bpermute(ms << 2, s0l), Lm = __builtin_amdgcn_ds_bpermute(ms << 2, Ll);
    const int mix = (mine >> 1) & 0x7fff, itop = (mine & 1) ? 2 * mix + 1 : mix;
    const int ta = mine < 0 ? 0 : (s0m + 32 * itop) * 1024, tb = mine < 0 ? 0 : (s0m + Lm) * 1024, tc = mine < 0 ? 0x7ffe : (itop << 1) | (mine & 1);
    unsigned long long fail;
    asm volatile(
      "s_mov_b32 s36, %[klo]\n\t"
      "s_mov_b32 s37, %[khi]\n\t"
      "s_mov_b32 s39, 0x20000\n\t"
      "s_mov_b32 s40, %[vlo]\n\t"
      "s_mov_b32 s41, %[vhi]\n\t"
      "s_mov_b32 s43, 0x20000\n\t"
      "s_mov_b32 s44, %[qlo]\n\t"
      "s_mov_b32 s45, %[qhi]\n\t"
      "s_mov_b32 s47, 0x20000\n\t"
      "s_mov_b32 s54, %[woff]\n\t"
      "s_lshl_b32 s83, %[woff], 2\n\t"
      "s_add_u32 s83, s83, 98304\n\t"
      "v_mov_b32 v43, 0x80000000\n\t"
      "s_mov_b32 s48, %[olo]\n\t"
      "s_mov_b32 s49, %[ohi]\n\t"
      "s_mov_b32 s50, 0\n\t"
      "s_mov_b32 s51, 0x20000\n\t"
      "s_mov_b32 s61, 0\n\t"
      "s_mov_b32 s60, 0\n\t"
      "s_mov_b32 s64, %[n]\n\t"
      "s_mov_b32 s62, 0x3e0293ee\n\t"
      "s_mov_b32 s63, %[thr]\n\t"
      "s_mov_b64 s[74:75], 0\n\t"
      "s_mov_b32 s59, -1\n\t"
      "s_mov_b32 s57, -1\n\t"
      "s_mov_b32 s56, 0\n\t"
      "s_mov_b32 s55, 0\n\t"
      "s_mov_b32 s87, -1\n\t"
      "s_mov_b32 s86, 0\n\t"
      "s_add_u32 s55, s55, s86\n\t"
      "s_sub_u32 s56, s56, 1\n\t"
      "s_cbranch_scc1 Lps19_%=\n\t"
      "Lpr19_%=:\n\t"
      "s_add_u32 s68, s59, 1\n\t"
      "v_readlane_b32 s65, %[ta], s68\n\t"
      "v_readlane_b32 s66, %[tb], s68\n\t"
      "v_readlane_b32 s67, %[tc], s68\n\t"
      "s_lshl_b32 s65, s65, 2\n\t"
      "s_lshl_b32 s66, s66, 2\n\t"
      "s_nop 4\n\t"
      "s_mov_b32 s46, s66\n\t"
      "s_cmp_lg_u32 %[early], 0\n\t"
      "s_cbranch_scc1 Learly0%=\n\t"
      "s_add_u32 m0, s54, 0\n\t"
      "s_nop 0\n\t"
      "buffer_load_dwordx4 %[vk0], s[36:39], s55 offen lds\n\t"
      "buffer_load_dwordx4 %[vk1], s[36:39], s55 offen offset:1024 lds\n\t"
      "s_mov_b32 s46, s66\n\t"
      "s_mov_b32 s69, s65\n\t"
      "s_add_u32 m0, s83, 8192\n\t"
      "s_nop 0\n\t"
      "buffer_load_dwordx4 %[vqa], s[44:47], s69 offen offset:0  lds\n\t"
      "s_add_u32 s69, s69, 15360\n\t"
      "buffer_load_dwordx4 %[vqa], s[44:47], s69 offen offset:1024  lds\n\t"
      "s_add_u32 s69, s69, 15360\n\t"
      "buffer_load_dwordx4 %[vqb], s[44:47], s69 offen offset:2048  lds\n\t"
      "s_add_u32 s69, s69, 15360\n\t"
      "buffer_load_dwordx4 %[vqb], s[44:47], s69 offen offset:3072  lds\n\t"
      "s_add_u32 s69, s69, 19456\n\t"
      "s_add_u32 m0, s83, 12288\n\t"
      "s_nop 0\n\t"
      "buffer_load_dwordx4 %[vqa], s[44:47], s69 offen offset:0  lds\n\t"
      "s_add_u32 s69, s69, 15360\n\t"
      "buffer_load_dwordx4 %[vqa], s[44:47], s69 offen offset:1024  lds\n\t"
      "s_add_u32 s69, s69, 15360\n\t"
      "buffer_load_dwordx4 %[vqb], s[44:47], s69 offen offset:2048  lds\n\t"
      "s_add_u32 s69, s69, 15360\n\t"
      "buffer_load_dwordx4 %[vqb], s[44:47], s69 offen offset:3072  lds\n\t"
      "s_add_u32 s69, s69, 19456\n\t"
      "s_add_u32 m0, s54, 0\n\t"
      "s_nop 0\n\t"
      "buffer_load_dwordx4 %[vv0], s[40:43], s55 offen offset:2048 lds\n\t"
      "buffer_load_dwordx4 %[vv1], s[40:43], s55 offen offset:3072 lds\n\t"
      "Learly0%=:\n\t"
      "s_add_u32 s55, s55, s86\n\t"
      "s_sub_u32 s56, s56, 1\n\t"
      "s_cbranch_scc1 Lps20_%=\n\t"
      "Lpr20_%=:\n\t"
      "s_nop 4\n\t"
      "s_waitcnt vmcnt(6)\n\t"
      "s_add_u32 m0, s54, 16384\n\t"
      "s_nop 0\n\t"
      "buffer_load_dwordx4 %[vk0], s[36:39], s55 offen lds\n\t"
      "buffer_load_dwordx4 %[vk1], s[36:39], s55 offen offset:1024 lds\n\t"
      "buffer_load_dwordx4 %[vv0], s[40:43], s55 offen offset:2048 lds\n\t"
      "buffer_load_dwordx4 %[vv1], s[40:43], s55 offen offset:3072 lds\n\t"
      "s_sub_u32 s69, s65, 0x20000\n\t"
      "s_bitcmp1_b32 s67, 0\n\t"
      "s_cselect_b32 s69, s69, 0x7f000000\n\t"
      "s_add_u32 m0, s83, 0\n\t"
      "s_nop 0\n\t"
      "buffer_load_dwordx4 %[vqa], s[44:47], s69 offen offset:0  lds\n\t"
      "s_add_u32 s69, s69, 15360\n\t"
      "buffer_load_dwordx4 %[vqa], s[44:47], s69 offen offset:1024  lds\n\t"
      "s_add_u32 s69, s69, 15360\n\t"
      "buffer_load_dwordx4 %[vqb], s[44:47], s69 offen offset:2048  lds\n\t"
      "s_add_u32 s69, s69, 15360\n\t"
      "buffer_load_dwordx4 %[vqb], s[44:47], s69 offen offset:3072  lds\n\t"
      "s_add_u32 s69, s69, 19456\n\t"
      "s_add_u32 m0, s83, 4096\n\t"
      "s_nop 0\n\t"
      "buffer_load_dwordx4 %[vqa], s[44:47], s69 offen offset:0  lds\n\t"
      "s_add_u32 s69, s69, 15360\n\t"
      "buffer_load_dwordx4 %[vqa], s[44:47], s69 offen offset:1024  lds\n\t"
      "s_add_u32 s69, s69, 15360\n\t"
      "buffer_load_dwordx4 %[vqb], s[44:47], s69 offen offset:2048  lds\n\t"
      "s_add_u32 s69, s69, 15360\n\t"
      "buffer_load_dwordx4 %[vqb], s[44:47], s69 offen offset:3072  lds\n\t"
      "s_add_u32 s69, s69, 19456\n\t"
      "s_add_u32 s55, s55, s86\n\t"
      "s_sub_u32 s56, s56, 1\n\t"
      "s_cbranch_scc1 Lps21_%=\n\t"
      "Lpr21_%=:\n\t"
      "s_nop 4\n\t"
      "s_add_u32 m0, s54, 32768\n\t"
      "s_nop 0\n\t"
      "buffer_load_dwordx4 %[vk0], s[36:39], s55 offen lds\n\t"
      "buffer_load_dwordx4 %[vk1], s[36:39], s55 offen offset:1024 lds\n\t"
      "buffer_load_dwordx4 %[vv0], s[40:43], s55 offen offset:2048 lds\n\t"
      "buffer_load_dwordx4 %[vv1], s[40:43], s55 offen offset:3072 lds\n\t"
      "s_add_u32 s55, s55, s86\n\t"
      "s_sub_u32 s56, s56, 1\n\t"
      "s_cbranch_scc1 Lps22_%=\n\t"
      "Lpr22_%=:\n\t"
      "s_nop 4\n\t"
      "s_add_u32 m0, s54, 49152\n\t"
      "s_nop 0\n\t"
      "buffer_load_dwordx4 %[vk0], s[36:39], s55 offen lds\n\t"
      "buffer_load_dwordx4 %[vk1], s[36:39], s55 offen offset:1024 lds\n\t"
      "buffer_load_dwordx4 %[vv0], s[40:43], s55 offen offset:2048 lds\n\t"
      "buffer_load_dwordx4 %[vv1], s[40:43], s55 offen offset:3072 lds\n\t"
      "s_add_u32 s55, s55, s86\n\t"
      "s_sub_u32 s56, s56, 1\n\t"
      "s_cbranch_scc1 Lps23_%=\n\t"
      "Lpr23_%=:\n\t"
      "s_nop 4\n\t"
      "s_add_u32 m0, s54, 65536\n\t"
      "s_nop 0\n\t"
      "buffer_load_dwordx4 %[vk0], s[36:39], s55 offen lds\n\t"
      "buffer_load_dwordx4 %[vk1], s[36:39], s55 offen offset:1024 lds\n\t"
      "buffer_load_dwordx4 %[vv0], s[40:43], s55 offen offset:2048 lds\n\t"
      "buffer_load_dwordx4 %[vv1], s[40:43], s55 offen offset:3072 lds\n\t"
      "s_add_u32 s55, s55, s86\n\t"
      "s_sub_u32 s56, s56, 1\n\t"
      "s_cbranch_scc1 Lps24_%=\n\t"
      "Lpr24_%=:\n\t"
      "s_nop 4\n\t"
      "s_mov_b32 s52, 81920\n\t"
      "s_mov_b32 s53, 0\n\t"
      "v_mov_b32 v40, %[kb]\n\t"
      "v_add_u32 v53, 0, %[vo]\n\t"
      "v_add_u32 v54, 16384, %[vo]\n\t"
      "v_add_u32 v55, 32768, %[vo]\n\t"
      "v_add_u32 v56, 49152, %[vo]\n\t"
      "v_xor_b32 v112, 0, %[wa]\n\t"
      "v_xor_b32 v113, 32, %[wa]\n\t"
      "v_xor_b32 v114, 64, %[wa]\n\t"
      "v_xor_b32 v115, 96, %[wa]\n\t"
      "v_xor_b32 v116, 128, %[wa]\n\t"
      "v_xor_b32 v117, 160, %[wa]\n\t"
      "v_xor_b32 v118, 192, %[wa]\n\t"
      "v_xor_b32 v119, 224, %[wa]\n\t"
      "v_xor_b32 v120, 0, %[ra]\n\t"
      "v_xor_b32 v121, 1088, %[ra]\n\t"
      "v_xor_b32 v122, 2176, %[ra]\n\t"
      "v_xor_b32 v123, 3264, %[ra]\n\t"
      "v_mov_b32 v44, 0xff800000\n\t"
      "v_cmp_gt_i32 vcc, 0, %[md]\n\t"
      "s_nop 1\n\t"
      "v_cndmask_b32 v32, 0, v44, vcc\n\t"
      "v_mov_b32 v44, 0xff800000\n\t"
      "v_cmp_gt_i32 vcc, 1, %[md]\n\t"
      "s_nop 1\n\t"
      "v_cndmask_b32 v33, 0, v44, vcc\n\t"
      "v_mov_b32 v44, 0xff800000\n\t"
      "v_cmp_gt_i32 vcc, 2, %[md]\n\t"
      "s_nop 1\n\t"
      "v_cndmask_b32 v34, 0, v44, vcc\n\t"
      "v_mov_b32 v44, 0xff800000\n\t"
      "v_cmp_gt_i32 vcc, 3, %[md]\n\t"
      "s_nop 1\n\t"
      "v_cndmask_b32 v35, 0, v44, vcc\n\t"
      "v_mov_b32 v44, 0x3f803f80\n\t"
      "v_accvgpr_write_b32 a128, v44\n\t"
      "v_accvgpr_write_b32 a129, v44\n\t"
      "v_accvgpr_write_b32 a130, v44\n\t"
      "v_accvgpr_write_b32 a131, v44\n\t"
      "s_waitcnt vmcnt(26)\n\t"
      "s_barrier\n\t"
      "ds_read_b128 a[64:67], v40 offset:0\n\t"
      "ds_read_b128 a[68:71], v40 offset:256\n\t"
      "ds_read_b128 a[72:75], v40 offset:512\n\t"
      "ds_read_b128 a[76:79], v40 offset:768\n\t"
      "ds_read_b128 a[80:83], v40 offset:8192\n\t"
      "ds_read_b128 a[84:87], v40 offset:8448\n\t"
      "ds_read_b128 a[88:91], v40 offset:8704\n\t"
      "ds_read_b128 a[92:95], v40 offset:8960\n\t"
      "ds_read_b128 a[32:35], %[qb] offset:8192\n\t"
      "ds_read_b128 a[36:39], %[qb] offset:8448\n\t"
      "ds_read_b128 a[40:43], %[qb] offset:8704\n\t"
      "ds_read_b128 a[44:47], %[qb] offset:8960\n\t"
      "ds_read_b128 a[48:51], %[qb] offset:12288\n\t"
      "ds_read_b128 a[52:55], %[qb] offset:12544\n\t"
      "ds_read_b128 a[56:59], %[qb] offset:12800\n\t"
      "ds_read_b128 a[60:63], %[qb] offset:13056\n\t"
      "Lnext%=:\n\t"
      "s_add_u32 s59, s59, 1\n\t"
      "s_cmp_ge_u32 s59, s64\n\t"
      "s_cbranch_scc1 Lend%=\n\t"
      "s_mov_b32 s78, s65\n\t"
      "s_mov_b32 s82, s66\n\t"
      "s_lshr_b32 s58, s67, 1\n\t"
      "s_and_b32 s70, s67, 1\n\t"
      "s_add_u32 s68, s59, 1\n\t"
      "v_readlane_b32 s65, %[ta], s68\n\t"
      "v_readlane_b32 s66, %[tb], s68\n\t"
      "v_readlane_b32 s67, %[tc], s68\n\t"
      "s_lshl_b32 s65, s65, 2\n\t"
      "s_lshl_b32 s66, s66, 2\n\t"
      "s_mov_b32 s46, s66\n\t"
      "s_mov_b32 s92, s65\n\t"
      "s_add_u32 s91, s83, 8192\n\t"
      "s_and_b32 s89, s67, 1\n\t"
      "s_add_u32 s89, s89, 1\n\t"
      "s_cmp_eq_u32 s66, 0\n\t"
      "s_cselect_b32 s89, 0, s89\n\t"
      "s_cmp_eq_u32 s70, 0\n\t"
      "s_cbranch_scc1 Lnxs%=\n\t"
      "s_sub_u32 s58, s58, 1\n\t"
      "s_sub_u32 s68, s58, s89\n\t"
      "s_sub_u32 s69, s68, 0\n\t"
      "s_min_i32 s90, s69, 2\n\t"
      "s_sub_u32 s68, s68, s90\n\t"
      "s_cmp_ge_i32 s69, 0\n\t"
      "s_cselect_b32 s93, 0, s89\n\t"
      "s_cselect_b32 s89, s89, 0\n\t"
      "s_cmp_lg_u32 s89, 0\n\t"
      "s_cselect_b32 s60, s68, s58\n\t"
      "Lhd%=:\n\t"
      "s_add_u32 m0, s52, s54\n\t"
      "s_mov_b32 s52, s53\n\t"
      "s_add_u32 s53, s53, 16384\n\t"
      "s_cmp_eq_u32 s53, 98304\n\t"
      "s_cselect_b32 s53, 0, s53\n\t"
      "v_add_u32 v41, s52, %[vb0]\n\t"
      "v_add_u32 v42, s52, %[vb1]\n\t"
      "v_add_u32 v40, s53, %[kb]\n\t"
      "s_waitcnt vmcnt(12)\n\t"
      "s_barrier\n\t"
      "s_waitcnt lgkmcnt(7)\n\t"
      "v_mfma_f32_16x16x32_bf16 v[80:83], a[64:67], a[32:35], v[32:35]\n\t"
      "ds_read_b128 a[0:3], %[qb] offset:0\n\t"
      "buffer_load_dwordx4 %[vk0], s[36:39], s55 offen lds\n\t"
      "s_waitcnt lgkmcnt(7)\n\t"
      "v_mfma_f32_16x16x32_bf16 v[80:83], a[68:71], a[36:39], v[80:83]\n\t"
      "ds_read_b128 a[4:7], %[qb] offset:256\n\t"
      "buffer_load_dwordx4 %[vk1], s[36:39], s55 offen offset:1024 lds\n\t"
      "s_waitcnt lgkmcnt(2)\n\t"
      "v_mfma_f32_16x16x32_bf16 v[80:83], a[72:75], a[40:43], v[80:83]\n\t"
      "ds_read_b128 a[8:11], %[qb] offset:512\n\t"
      "buffer_load_dwordx4 %[vv0], s[40:43], s55 offen offset:2048 lds\n\t"
      "v_mfma_f32_16x16x32_bf16 v[80:83], a[76:79], a[44:47], v[80:83]\n\t"
      "ds_read_b128 a[12:15], %[qb] offset:768\n\t"
      "buffer_load_dwordx4 %[vv1], s[40:43], s55 offen offset:3072 lds\n\t"
      "s_add_u32 s55, s55, s86\n\t"
      "v_mfma_f32_16x16x32_bf16 v[88:91], a[64:67], a[48:51], 0\n\t"
      "ds_read_b128 a[16:19], %[qb] offset:4096\n\t"
      "s_sub_u32 s56, s56, 1\n\t"
      "s_cbranch_scc1 Lps2_%=\n\t"
      "Lpr2_%=:\n\t"
      "v_mfma_f32_16x16x32_bf16 v[88:91], a[68:71], a[52:55], v[88:91]\n\t"
      "ds_read_b128 a[20:23], %[qb] offset:4352\n\t"
      "s_cmp_lg_u32 s93, 0\n\t"
      "v_max3_f32 v44, v80, v81, v82\n\t"
      "v_mfma_f32_16x16x32_bf16 v[88:91], a[72:75], a[56:59], v[88:91]\n\t"
      "ds_read_b128 a[24:27], %[qb] offset:4608\n\t"
      "v_max_f32 v44, v44, v83\n\t"
      "v_mfma_f32_16x16x32_bf16 v[88:91], a[76:79], a[60:63], v[88:91]\n\t"
      "ds_read_b128 a[28:31], %[qb] offset:4864\n\t"
      "s_cbranch_scc1 Lqs3_%=\n\t"
      "Lqr3_%=:\n\t"
      "v_mov_b32 v45, v44\n\t"
      "v_mfma_f32_16x16x32_bf16 v[92:95], a[80:83], a[48:51], v[32:35]\n\t"
      "ds_read_b128 a[64:67], v40 offset:0\n\t"
      "s_add_u32 s70, s61, 0x10000\n\t"
      "s_add_u32 m0, s52, s54\n\t"
      "s_mov_b32 s52, s53\n\t"
      "s_add_u32 s53, s53, 16384\n\t"
      "s_cmp_eq_u32 s53, 98304\n\t"
      "s_cselect_b32 s53, 0, s53\n\t"
      "v_mfma_f32_16x16x32_bf16 v[92:95], a[84:87], a[52:55], v[92:95]\n\t"
      "ds_read_b128 a[68:71], v40 offset:256\n\t"
      "buffer_store_dwordx4 v[132:135], v53, s[48:51], s61 offen sc1 nt\n\t"
      "v_mfma_f32_16x16x32_bf16 v[92:95], a[88:91], a[56:59], v[92:95]\n\t"
      "ds_read_b128 a[72:75], v40 offset:512\n\t"
      "v_permlane16_swap_b32 v44, v45\n\t"
      "v_mfma_f32_16x16x32_bf16 v[92:95], a[92:95], a[60:63], v[92:95]\n\t"
      "ds_read_b128 a[76:79], v40 offset:768\n\t"
      "v_max_f32 v44, v44, v45\n\t"
      "v_mov_b32 v45, v44\n\t"
      "v_max3_f32 v46, v88, v89, v90\n\t"
      "v_mov_b32 v106, 0\n\t"
      "v_permlane32_swap_b32 v44, v45\n\t"
      "v_max_f32 v44, v44, v45\n\t"
      "v_mul_f32 v38, 0xbe0293ee, v44\n\t"
      "v_max3_f32 v46, v46, v91, v92\n\t"
      "v_max3_f32 v46, v46, v93, v94\n\t"
      "v_max_f32 v46, v46, v95\n\t"
      "v_mov_b32 v47, v46\n\t"
      "v_fma_f32 v80, v80, s62, v38\n\t"
      "v_fma_f32 v81, v81, s62, v38\n\t"
      "v_permlane16_swap_b32 v46, v47\n\t"
      "s_waitcnt lgkmcnt(3)\n\t"
      "v_mfma_f32_16x16x32_bf16 v[64:67], a[64:67], a[0:3], v[32:35]\n\t"
      "ds_read_b128 a[80:83], v40 offset:8192\n\t"
      "buffer_store_dwordx4 v[140:143], v54, s[48:51], s61 offen sc1 nt\n\t"
      "v_max_f32 v46, v46, v47\n\t"
      "v_mov_b32 v47, v46\n\t"
      "v_exp_f32 v80, v80\n\t"
      "s_waitcnt lgkmcnt(3)\n\t"
      "v_mfma_f32_16x16x32_bf16 v[64:67], a[68:71], a[4:7], v[64:67]\n\t"
      "ds_read_b128 a[84:87], v40 offset:8448\n\t"
      "v_permlane32_swap_b32 v46, v47\n\t"
      "s_waitcnt lgkmcnt(3)\n\t"
      "v_mfma_f32_16x16x32_bf16 v[64:67], a[72:75], a[8:11], v[64:67]\n\t"
      "ds_read_b128 a[88:91], v40 offset:8704\n\t"
      "v_max_f32 v46, v46, v47\n\t"
      "s_waitcnt lgkmcnt(3)\n\t"
      "v_mfma_f32_16x16x32_bf16 v[64:67], a[76:79], a[12:15], v[64:67]\n\t"
      "ds_read_b128 a[92:95], v40 offset:8960\n\t"
      "v_add_u32 v40, s53, %[kb]\n\t"
      "v_mfma_f32_16x16x32_bf16 v[72:75], a[64:67], a[16:19], 0\n\t"
      "ds_read_b64_tr_b16 a[96:97], v41 offset:0\n\t"
      "ds_read_b64_tr_b16 a[98:99], v41 offset:8192\n\t"
      "v_mfma_f32_16x16x32_bf16 v[72:75], a[68:71], a[20:23], v[72:75]\n\t"
      "ds_read_b64_tr_b16 a[100:101], v42 offset:0\n\t"
      "ds_read_b64_tr_b16 a[102:103], v42 offset:8192\n\t"
      "v_mfma_f32_16x16x32_bf16 v[72:75], a[72:75], a[24:27], v[72:75]\n\t"
      "ds_read_b64_tr_b16 a[104:105], v41 offset:512\n\t"
      "ds_read_b64_tr_b16 a[106:107], v41 offset:8704\n\t"
      "v_mfma_f32_16x16x32_bf16 v[72:75], a[76:79], a[28:31], v[72:75]\n\t"
      "ds_read_b64_tr_b16 a[108:109], v42 offset:512\n\t"
      "ds_read_b64_tr_b16 a[110:111], v42 offset:8704\n\t"
      "s_waitcnt lgkmcnt(11)\n\t"
      "v_mfma_f32_16x16x32_bf16 v[76:79], a[80:83], a[16:19], v[32:35]\n\t"
      "ds_read_b64_tr_b16 a[112:113], v41 offset:1024\n\t"
      "ds_read_b64_tr_b16 a[114:115], v41 offset:9216\n\t"
      "ds_read_b64_tr_b16 a[116:117], v42 offset:1024\n\t"
      "s_waitcnt lgkmcnt(13)\n\t"
      "v_mfma_f32_16x16x32_bf16 v[76:79], a[84:87], a[20:23], v[76:79]\n\t"
      "ds_read_b64_tr_b16 a[118:119], v42 offset:9216\n\t"
      "ds_read_b64_tr_b16 a[120:121], v41 offset:1536\n\t"
      "s_waitcnt lgkmcnt(14)\n\t"
      "v_mfma_f32_16x16x32_bf16 v[76:79], a[88:91], a[24:27], v[76:79]\n\t"
      "ds_read_b64_tr_b16 a[122:123], v41 offset:9728\n\t"
      "ds_read_b64_tr_b16 a[124:125], v42 offset:1536\n\t"
      "s_waitcnt lgkmcnt(15)\n\t"
      "v_mfma_f32_16x16x32_bf16 v[76:79], a[92:95], a[28:31], v[76:79]\n\t"
      "ds_read_b64_tr_b16 a[126:127], v42 offset:9728\n\t"
      "buffer_store_dwordx4 v[148:151], v55, s[48:51], s61 offen sc1 nt\n\t"
      "v_add_u32 v41, s52, %[vb0]\n\t"
      "v_add_u32 v42, s52, %[vb1]\n\t"
      "v_max3_f32 v44, v64, v65, v66\n\t"
      "v_max_f32 v44, v44, v67\n\t"
      "v_mov_b32 v45, v44\n\t"
      "v_mul_f32 v39, 0xbe0293ee, v46\n\t"
      "v_max3_f32 v46, v72, v73, v74\n\t"
      "v_permlane16_swap_b32 v44, v45\n\t"
      "v_max_f32 v44, v44, v45\n\t"
      "v_mov_b32 v45, v44\n\t"
      "v_max3_f32 v46, v46, v75, v76\n\t"
      "v_max3_f32 v46, v46, v77, v78\n\t"
      "v_permlane32_swap_b32 v44, v45\n\t"
      "v_max_f32 v44, v44, v45\n\t"
      "v_mul_f32 v36, 0xbe0293ee, v44\n\t"
      "v_max_f32 v46, v46, v79\n\t"
      "v_mov_b32 v47, v46\n\t"
      "v_exp_f32 v81, v81\n\t"
      "s_nop 0\n\t"
      "v_permlane16_swap_b32 v46, v47\n\t"
      "v_max_f32 v46, v46, v47\n\t"
      "v_mov_b32 v47, v46\n\t"
      "buffer_store_dwordx4 v[156:159], v56, s[48:51], s61 offen sc1 nt\n\t"
      "s_mov_b32 s61, s78\n\t"
      "v_permlane32_swap_b32 v46, v47\n\t"
      "v_max_f32 v46, v46, v47\n\t"
      "v_mul_f32 v37, 0xbe0293ee, v46\n\t"
      "v_cvt_pk_bf16_f32 v104, v80, v81\n\t"
      "v_fma_f32 v82, v82, s62, v38\n\t"
      "v_fma_f32 v83, v83, s62, v38\n\t"
      "v_exp_f32 v82, v82\n\t"
      "v_exp_f32 v83, v83\n\t"
      "v_fma_f32 v88, v88, s62, v39\n\t"
      "v_cvt_pk_bf16_f32 v105, v82, v83\n\t"
      "v_fma_f32 v89, v89, s62, v39\n\t"
      "v_exp_f32 v88, v88\n\t"
      "v_exp_f32 v89, v89\n\t"
      "v_fma_f32 v90, v90, s62, v39\n\t"
      "v_cvt_pk_bf16_f32 v108, v88, v89\n\t"
      "v_fma_f32 v91, v91, s62, v39\n\t"
      "v_exp_f32 v90, v90\n\t"
      "v_exp_f32 v91, v91\n\t"
      "v_fma_f32 v92, v92, s62, v39\n\t"
      "v_cvt_pk_bf16_f32 v109, v90, v91\n\t"
      "v_fma_f32 v93, v93, s62, v39\n\t"
      "v_exp_f32 v92, v92\n\t"
      "v_exp_f32 v93, v93\n\t"
      "v_fma_f32 v94, v94, s62, v39\n\t"
      "v_cvt_pk_bf16_f32 v110, v92, v93\n\t"
      "v_fma_f32 v95, v95, s62, v39\n\t"
      "v_exp_f32 v94, v94\n\t"
      "v_exp_f32 v95, v95\n\t"
      "v_mov_b32 v107, 0\n\t"
      "v_cvt_pk_bf16_f32 v111, v94, v95\n\t"
      "v_fma_f32 v64, v64, s62, v36\n\t"
      "s_waitcnt lgkmcnt(0)\n\t"
      "v_mfma_f32_16x16x32_bf16 v[192:195], a[96:99], v[104:107], 0\n\t"
      "buffer_store_dwordx4 v[164:167], v53, s[48:51], s70 offen sc1 nt\n\t"
      "v_fma_f32 v65, v65, s62, v36\n\t"
      "v_mfma_f32_16x16x32_bf16 v[196:199], a[100:103], v[104:107], 0\n\t"
      "v_exp_f32 v64, v64\n\t"
      "v_mfma_f32_16x16x32_bf16 v[200:203], a[104:107], v[104:107], 0\n\t"
      "v_exp_f32 v65, v65\n\t"
      "v_mfma_f32_16x16x32_bf16 v[204:207], a[108:111], v[104:107], 0\n\t"
      "v_cvt_pk_bf16_f32 v96, v64, v65\n\t"
      "v_fma_f32 v66, v66, s62, v36\n\t"
      "v_mfma_f32_16x16x32_bf16 v[208:211], a[112:115], v[104:107], 0\n\t"
      "v_fma_f32 v67, v67, s62, v36\n\t"
      "v_fma_f32 v72, v72, s62, v37\n\t"
      "v_mfma_f32_16x16x32_bf16 v[212:215], a[116:119], v[104:107], 0\n\t"
      "s_waitcnt vmcnt(17)\n\t"
      "v_exp_f32 v66, v66\n\t"
      "v_mfma_f32_16x16x32_bf16 v[216:219], a[120:123], v[104:107], 0\n\t"
      "s_barrier\n\t"
      "v_exp_f32 v67, v67\n\t"
      "v_mfma_f32_16x16x32_bf16 v[220:223], a[124:127], v[104:107], 0\n\t"
      "v_cvt_pk_bf16_f32 v97, v66, v67\n\t"
      "v_fma_f32 v73, v73, s62, v37\n\t"
      "v_mfma_f32_16x16x32_bf16 a[140:143], a[128:131], v[104:107], 0\n\t"
      "buffer_load_dwordx4 %[vk0], s[36:39], s55 offen lds\n\t"
      "v_fma_f32 v74, v74, s62, v37\n\t"
      "v_mfma_f32_16x16x32_bf16 v[224:227], a[96:99], v[108:111], 0\n\t"
      "ds_read_b64_tr_b16 a[96:97], v41 offset:0\n\t"
      "ds_read_b64_tr_b16 a[98:99], v41 offset:8192\n\t"
      "v_mfma_f32_16x16x32_bf16 v[228:231], a[100:103], v[108:111], 0\n\t"
      "ds_read_b64_tr_b16 a[100:101], v42 offset:0\n\t"
      "ds_read_b64_tr_b16 a[102:103], v42 offset:8192\n\t"
      "v_mfma_f32_16x16x32_bf16 v[232:235], a[104:107], v[108:111], 0\n\t"
      "ds_read_b64_tr_b16 a[104:105], v41 offset:512\n\t"
      "ds_read_b64_tr_b16 a[106:107], v41 offset:8704\n\t"
      "v_mfma_f32_16x16x32_bf16 v[236:239], a[108:111], v[108:111], 0\n\t"
      "ds_read_b64_tr_b16 a[108:109], v42 offset:512\n\t"
      "ds_read_b64_tr_b16 a[110:111], v42 offset:8704\n\t"
      "v_mfma_f32_16x16x32_bf16 v[240:243], a[112:115], v[108:111], 0\n\t"
      "ds_read_b64_tr_b16 a[112:113], v41 offset:1024\n\t"
      "ds_read_b64_tr_b16 a[114:115], v41 offset:9216\n\t"
      "v_mfma_f32_16x16x32_bf16 v[244:247], a[116:119], v[108:111], 0\n\t"
      "ds_read_b64_tr_b16 a[116:117], v42 offset:1024\n\t"
      "ds_read_b64_tr_b16 a[118:119], v42 offset:9216\n\t"
      "v_mfma_f32_16x16x32_bf16 v[248:251], a[120:123], v[108:111], 0\n\t"
      "ds_read_b64_tr_b16 a[120:121], v41 offset:1536\n\t"
      "ds_read_b64_tr_b16 a[122:123], v41 offset:9728\n\t"
      "v_mfma_f32_16x16x32_bf16 v[252:255], a[124:127], v[108:111], 0\n\t"
      "ds_read_b64_tr_b16 a[124:125], v42 offset:1536\n\t"
      "ds_read_b64_tr_b16 a[126:127], v42 offset:9728\n\t"
      "v_mfma_f32_16x16x32_bf16 a[144:147], a[128:131], v[108:111], 0\n\t"
      "buffer_load_dwordx4 %[vk1], s[36:39], s55 offen offset:1024 lds\n\t"
      "buffer_load_dwordx4 %[vv0], s[40:43], s55 offen offset:2048 lds\n\t"
      "v_mfma_f32_16x16x32_bf16 v[80:83], a[64:67], a[32:35], 0\n\t"
      "buffer_load_dwordx4 %[vv1], s[40:43], s55 offen offset:3072 lds\n\t"
      "buffer_store_dwordx4 v[172:175], v54, s[48:51], s70 offen sc1 nt\n\t"
      "v_mfma_f32_16x16x32_bf16 v[80:83], a[68:71], a[36:39], v[80:83]\n\t"
      "s_add_u32 s55, s55, s86\n\t"
      "v_exp_f32 v72, v72\n\t"
      "v_mfma_f32_16x16x32_bf16 v[80:83], a[72:75], a[40:43], v[80:83]\n\t"
      "s_sub_u32 s56, s56, 1\n\t"
      "v_exp_f32 v73, v73\n\t"
      "v_mfma_f32_16x16x32_bf16 v[80:83], a[76:79], a[44:47], v[80:83]\n\t"
      "s_cbranch_scc1 Lps5_%=\n\t"
      "Lpr5_%=:\n\t"
      "v_cvt_pk_bf16_f32 v100, v72, v73\n\t"
      "v_fma_f32 v75, v75, s62, v37\n\t"
      "v_mfma_f32_16x16x32_bf16 v[84:87], a[80:83], a[32:35], 0\n\t"
      "v_exp_f32 v74, v74\n\t"
      "v_mfma_f32_16x16x32_bf16 v[84:87], a[84:87], a[36:39], v[84:87]\n\t"
      "buffer_store_dwordx4 v[180:183], v55, s[48:51], s70 offen sc1 nt\n\t"
      "v_mfma_f32_16x16x32_bf16 v[84:87], a[88:91], a[40:43], v[84:87]\n\t"
      "v_exp_f32 v75, v75\n\t"
      "v_mfma_f32_16x16x32_bf16 v[84:87], a[92:95], a[44:47], v[84:87]\n\t"
      "v_cvt_pk_bf16_f32 v101, v74, v75\n\t"
      "v_fma_f32 v76, v76, s62, v37\n\t"
      "v_mfma_f32_16x16x32_bf16 v[88:91], a[64:67], a[48:51], 0\n\t"
      "ds_read_b128 a[64:67], v40 offset:0\n\t"
      "v_fma_f32 v77, v77, s62, v37\n\t"
      "v_mfma_f32_16x16x32_bf16 v[88:91], a[68:71], a[52:55], v[88:91]\n\t"
      "ds_read_b128 a[68:71], v40 offset:256\n\t"
      "v_fma_f32 v78, v78, s62, v37\n\t"
      "v_mfma_f32_16x16x32_bf16 v[88:91], a[72:75], a[56:59], v[88:91]\n\t"
      "ds_read_b128 a[72:75], v40 offset:512\n\t"
      "buffer_store_dwordx4 v[188:191], v56, s[48:51], s70 offen sc1 nt\n\t"
      "v_mfma_f32_16x16x32_bf16 v[88:91], a[76:79], a[60:63], v[88:91]\n\t"
      "ds_read_b128 a[76:79], v40 offset:768\n\t"
      "v_fma_f32 v79, v79, s62, v37\n\t"
      "v_mfma_f32_16x16x32_bf16 v[92:95], a[80:83], a[48:51], 0\n\t"
      "ds_read_b128 a[80:83], v40 offset:8192\n\t"
      "v_mov_b32 v98, 0\n\t"
      "v_mfma_f32_16x16x32_bf16 v[92:95], a[84:87], a[52:55], v[92:95]\n\t"
      "ds_read_b128 a[84:87], v40 offset:8448\n\t"
      "v_mov_b32 v99, 0\n\t"
      "v_mfma_f32_16x16x32_bf16 v[92:95], a[88:91], a[56:59], v[92:95]\n\t"
      "ds_read_b128 a[88:91], v40 offset:8704\n\t"
      "v_fma_f32 v80, v80, s62, v38\n\t"
      "v_mfma_f32_16x16x32_bf16 v[92:95], a[92:95], a[60:63], v[92:95]\n\t"
      "ds_read_b128 a[92:95], v40 offset:8960\n\t"
      "v_fma_f32 v81, v81, s62, v38\n\t"
      "s_waitcnt lgkmcnt(8)\n\t"
      "v_mfma_f32_16x16x32_bf16 v[128:131], a[96:99], v[96:99], 0\n\t"
      "s_mov_b32 s50, s82\n\t"
      "v_exp_f32 v76, v76\n\t"
      "v_mfma_f32_16x16x32_bf16 v[132:135], a[100:103], v[96:99], 0\n\t"
      "v_exp_f32 v77, v77\n\t"
      "v_mfma_f32_16x16x32_bf16 v[136:139], a[104:107], v[96:99], 0\n\t"
      "v_cvt_pk_bf16_f32 v102, v76, v77\n\t"
      "v_fma_f32 v82, v82, s62, v38\n\t"
      "v_mfma_f32_16x16x32_bf16 v[140:143], a[108:111], v[96:99], 0\n\t"
      "v_exp_f32 v78, v78\n\t"
      "v_mfma_f32_16x16x32_bf16 v[144:147], a[112:115], v[96:99], 0\n\t"
      "v_exp_f32 v79, v79\n\t"
      "v_mfma_f32_16x16x32_bf16 v[148:151], a[116:119], v[96:99], 0\n\t"
      "v_cvt_pk_bf16_f32 v103, v78, v79\n\t"
      "v_fma_f32 v83, v83, s62, v38\n\t"
      "v_mfma_f32_16x16x32_bf16 v[152:155], a[120:123], v[96:99], 0\n\t"
      "v_exp_f32 v80, v80\n\t"
      "v_mfma_f32_16x16x32_bf16 v[156:159], a[124:127], v[96:99], 0\n\t"
      "v_exp_f32 v81, v81\n\t"
      "v_mfma_f32_16x16x32_bf16 a[132:135], a[128:131], v[96:99], 0\n\t"
      "v_cvt_pk_bf16_f32 v104, v80, v81\n\t"
      "v_fma_f32 v84, v84, s62, v38\n\t"
      "v_mfma_f32_16x16x32_bf16 v[160:163], a[96:99], v[100:103], 0\n\t"
      "v_exp_f32 v82, v82\n\t"
      "v_mfma_f32_16x16x32_bf16 v[164:167], a[100:103], v[100:103], 0\n\t"
      "v_exp_f32 v83, v83\n\t"
      "v_mfma_f32_16x16x32_bf16 v[168:171], a[104:107], v[100:103], 0\n\t"
      "v_cvt_pk_bf16_f32 v105, v82, v83\n\t"
      "v_fma_f32 v85, v85, s62, v38\n\t"
      "v_mfma_f32_16x16x32_bf16 v[172:175], a[108:111], v[100:103], 0\n\t"
      "v_exp_f32 v84, v84\n\t"
      "v_mfma_f32_16x16x32_bf16 v[176:179], a[112:115], v[100:103], 0\n\t"
      "v_exp_f32 v85, v85\n\t"
      "v_mfma_f32_16x16x32_bf16 v[180:183], a[116:119], v[100:103], 0\n\t"
      "v_cvt_pk_bf16_f32 v106, v84, v85\n\t"
      "v_fma_f32 v86, v86, s62, v38\n\t"
      "v_mfma_f32_16x16x32_bf16 v[184:187], a[120:123], v[100:103], 0\n\t"
      "v_fma_f32 v87, v87, s62, v38\n\t"
      "v_fma_f32 v88, v88, s62, v39\n\t"
      "v_mfma_f32_16x16x32_bf16 v[188:191], a[124:127], v[100:103], 0\n\t"
      "v_exp_f32 v86, v86\n\t"
      "v_mfma_f32_16x16x32_bf16 a[136:139], a[128:131], v[100:103], 0\n\t"
      "v_exp_f32 v87, v87\n\t"
      "v_fma_f32 v89, v89, s62, v39\n\t"
      "v_cvt_pk_bf16_f32 v107, v86, v87\n\t"
      "v_exp_f32 v88, v88\n\t"
      "v_exp_f32 v89, v89\n\t"
      "v_fma_f32 v90, v90, s62, v39\n\t"
      "v_cvt_pk_bf16_f32 v108, v88, v89\n\t"
      "v_fma_f32 v91, v91, s62, v39\n\t"
      "v_exp_f32 v90, v90\n\t"
      "v_exp_f32 v91, v91\n\t"
      "s_nop 0\n\t"
      "v_cvt_pk_bf16_f32 v109, v90, v91\n\t"
      "s_cmp_eq_u32 s60, 0\n\t"
      "s_cbranch_scc1 Lpd%=\n\t"
      "Led%=:\n\t"
      "s_sub_u32 s60, s60, 1\n\t"
      "s_min_u32 s58, s60, s56\n\t"
      "s_sub_u32 s60, s60, s58\n\t"
      "s_sub_u32 s56, s56, s58\n\t"
      "Lld%=:\n\t"
      "s_waitcnt lgkmcnt(0)\n\t"
      "v_mfma_f32_16x16x32_bf16 v[64:67], a[64:67], a[0:3], 0\n\t"
      "s_add_u32 m0, s52, s54\n\t"
      "v_fma_f32 v92, v92, s62, v39\n\t"
      "v_fma_f32 v93, v93, s62, v39\n\t"
      "v_mfma_f32_16x16x32_bf16 v[64:67], a[68:71], a[4:7], v[64:67]\n\t"
      "s_mov_b32 s52, s53\n\t"
      "v_add_u32 v41, s52, %[vb0]\n\t"
      "v_add_u32 v42, s52, %[vb1]\n\t"
      "v_mfma_f32_16x16x32_bf16 v[64:67], a[72:75], a[8:11], v[64:67]\n\t"
      "s_add_u32 s53, s53, 16384\n\t"
      "v_exp_f32 v92, v92\n\t"
      "v_mfma_f32_16x16x32_bf16 v[64:67], a[76:79], a[12:15], v[64:67]\n\t"
      "s_cmp_eq_u32 s53, 98304\n\t"
      "v_exp_f32 v93, v93\n\t"
      "v_mfma_f32_16x16x32_bf16 v[68:71], a[80:83], a[0:3], 0\n\t"
      "s_cselect_b32 s53, 0, s53\n\t"
      "v_add_u32 v40, s53, %[kb]\n\t"
      "v_cvt_pk_bf16_f32 v110, v92, v93\n\t"
      "v_mfma_f32_16x16x32_bf16 v[68:71], a[84:87], a[4:7], v[68:71]\n\t"
      "v_fma_f32 v94, v94, s62, v39\n\t"
      "v_fma_f32 v95, v95, s62, v39\n\t"
      "v_mfma_f32_16x16x32_bf16 v[68:71], a[88:91], a[8:11], v[68:71]\n\t"
      "v_exp_f32 v94, v94\n\t"
      "v_mfma_f32_16x16x32_bf16 v[68:71], a[92:95], a[12:15], v[68:71]\n\t"
      "v_exp_f32 v95, v95\n\t"
      "v_mfma_f32_16x16x32_bf16 v[72:75], a[64:67], a[16:19], 0\n\t"
      "v_cvt_pk_bf16_f32 v111, v94, v95\n\t"
      "v_fma_f32 v64, v64, s62, v36\n\t"
      "v_mfma_f32_16x16x32_bf16 v[72:75], a[68:71], a[20:23], v[72:75]\n\t"
      "v_fma_f32 v65, v65, s62, v36\n\t"
      "v_fma_f32 v66, v66, s62, v36\n\t"
      "v_mfma_f32_16x16x32_bf16 v[72:75], a[72:75], a[24:27], v[72:75]\n\t"
      "v_exp_f32 v64, v64\n\t"
      "v_mfma_f32_16x16x32_bf16 v[72:75], a[76:79], a[28:31], v[72:75]\n\t"
      "v_exp_f32 v65, v65\n\t"
      "v_mfma_f32_16x16x32_bf16 v[76:79], a[80:83], a[16:19], 0\n\t"
      "v_cvt_pk_bf16_f32 v96, v64, v65\n\t"
      "v_fma_f32 v67, v67, s62, v36\n\t"
      "v_mfma_f32_16x16x32_bf16 v[76:79], a[84:87], a[20:23], v[76:79]\n\t"
      "v_exp_f32 v66, v66\n\t"
      "v_mfma_f32_16x16x32_bf16 v[76:79], a[88:91], a[24:27], v[76:79]\n\t"
      "v_exp_f32 v67, v67\n\t"
      "v_mfma_f32_16x16x32_bf16 v[76:79], a[92:95], a[28:31], v[76:79]\n\t"
      "v_cvt_pk_bf16_f32 v97, v66, v67\n\t"
      "v_fma_f32 v68, v68, s62, v36\n\t"
      "v_mfma_f32_16x16x32_bf16 v[192:195], a[96:99], v[104:107], v[192:195]\n\t"
      "v_fma_f32 v69, v69, s62, v36\n\t"
      "v_fma_f32 v70, v70, s62, v36\n\t"
      "v_mfma_f32_16x16x32_bf16 v[196:199], a[100:103], v[104:107], v[196:199]\n\t"
      "v_exp_f32 v68, v68\n\t"
      "v_mfma_f32_16x16x32_bf16 v[200:203], a[104:107], v[104:107], v[200:203]\n\t"
      "v_exp_f32 v69, v69\n\t"
      "v_mfma_f32_16x16x32_bf16 v[204:207], a[108:111], v[104:107], v[204:207]\n\t"
      "v_cvt_pk_bf16_f32 v98, v68, v69\n\t"
      "v_fma_f32 v71, v71, s62, v36\n\t"
      "v_mfma_f32_16x16x32_bf16 v[208:211], a[112:115], v[104:107], v[208:211]\n\t"
      "v_exp_f32 v70, v70\n\t"
      "v_mfma_f32_16x16x32_bf16 v[212:215], a[116:119], v[104:107], v[212:215]\n\t"
      "s_waitcnt vmcnt(12)\n\t"
      "v_exp_f32 v71, v71\n\t"
      "v_mfma_f32_16x16x32_bf16 v[216:219], a[120:123], v[104:107], v[216:219]\n\t"
      "s_barrier\n\t"
      "v_cvt_pk_bf16_f32 v99, v70, v71\n\t"
      "v_fma_f32 v72, v72, s62, v37\n\t"
      "v_mfma_f32_16x16x32_bf16 v[220:223], a[124:127], v[104:107], v[220:223]\n\t"
      "v_fma_f32 v73, v73, s62, v37\n\t"
      "v_fma_f32 v74, v74, s62, v37\n\t"
      "v_mfma_f32_16x16x32_bf16 a[140:143], a[128:131], v[104:107], a[140:143]\n\t"
      "v_exp_f32 v72, v72\n\t"
      "v_mfma_f32_16x16x32_bf16 v[224:227], a[96:99], v[108:111], v[224:227]\n\t"
      "ds_read_b64_tr_b16 a[96:97], v41 offset:0\n\t"
      "v_fma_f32 v75, v75, s62, v37\n\t"
      "v_mfma_f32_16x16x32_bf16 v[228:231], a[100:103], v[108:111], v[228:231]\n\t"
      "ds_read_b64_tr_b16 a[98:99], v41 offset:8192\n\t"
      "buffer_load_dwordx4 %[vk0], s[36:39], s55 offen lds\n\t"
      "v_mfma_f32_16x16x32_bf16 v[232:235], a[104:107], v[108:111], v[232:235]\n\t"
      "ds_read_b64_tr_b16 a[100:101], v42 offset:0\n\t"
      "v_fma_f32 v76, v76, s62, v37\n\t"
      "v_mfma_f32_16x16x32_bf16 v[236:239], a[108:111], v[108:111], v[236:239]\n\t"
      "ds_read_b64_tr_b16 a[102:103], v42 offset:8192\n\t"
      "v_fma_f32 v77, v77, s62, v37\n\t"
      "v_mfma_f32_16x16x32_bf16 v[240:243], a[112:115], v[108:111], v[240:243]\n\t"
      "ds_read_b64_tr_b16 a[104:105], v41 offset:512\n\t"
      "v_fma_f32 v78, v78, s62, v37\n\t"
      "v_mfma_f32_16x16x32_bf16 v[244:247], a[116:119], v[108:111], v[244:247]\n\t"
      "ds_read_b64_tr_b16 a[106:107], v41 offset:8704\n\t"
      "v_fma_f32 v79, v79, s62, v37\n\t"
      "v_mfma_f32_16x16x32_bf16 v[248:251], a[120:123], v[108:111], v[248:251]\n\t"
      "ds_read_b64_tr_b16 a[108:109], v42 offset:512\n\t"
      "v_exp_f32 v73, v73\n\t"
      "v_mfma_f32_16x16x32_bf16 v[252:255], a[124:127], v[108:111], v[252:255]\n\t"
      "ds_read_b64_tr_b16 a[110:111], v42 offset:8704\n\t"
      "v_cvt_pk_bf16_f32 v100, v72, v73\n\t"
      "v_mfma_f32_16x16x32_bf16 a[144:147], a[128:131], v[108:111], a[144:147]\n\t"
      "ds_read_b64_tr_b16 a[112:113], v41 offset:1024\n\t"
      "v_exp_f32 v74, v74\n\t"
      "v_mfma_f32_16x16x32_bf16 v[80:83], a[64:67], a[32:35], 0\n\t"
      "ds_read_b64_tr_b16 a[114:115], v41 offset:9216\n\t"
      "v_exp_f32 v75, v75\n\t"
      "v_mfma_f32_16x16x32_bf16 v[80:83], a[68:71], a[36:39], v[80:83]\n\t"
      "ds_read_b64_tr_b16 a[116:117], v42 offset:1024\n\t"
      "v_cvt_pk_bf16_f32 v101, v74, v75\n\t"
      "v_mfma_f32_16x16x32_bf16 v[80:83], a[72:75], a[40:43], v[80:83]\n\t"
      "ds_read_b64_tr_b16 a[118:119], v42 offset:9216\n\t"
      "buffer_load_dwordx4 %[vk1], s[36:39], s55 offen offset:1024 lds\n\t"
      "v_mfma_f32_16x16x32_bf16 v[80:83], a[76:79], a[44:47], v[80:83]\n\t"
      "ds_read_b64_tr_b16 a[120:121], v41 offset:1536\n\t"
      "v_exp_f32 v76, v76\n\t"
      "v_mfma_f32_16x16x32_bf16 v[84:87], a[80:83], a[32:35], 0\n\t"
      "ds_read_b64_tr_b16 a[122:123], v41 offset:9728\n\t"
      "v_exp_f32 v77, v77\n\t"
      "v_mfma_f32_16x16x32_bf16 v[84:87], a[84:87], a[36:39], v[84:87]\n\t"
      "ds_read_b64_tr_b16 a[124:125], v42 offset:1536\n\t"
      "v_cvt_pk_bf16_f32 v102, v76, v77\n\t"
      "v_mfma_f32_16x16x32_bf16 v[84:87], a[88:91], a[40:43], v[84:87]\n\t"
      "ds_read_b64_tr_b16 a[126:127], v42 offset:9728\n\t"
      "v_fma_f32 v80, v80, s62, v38\n\t"
      "v_mfma_f32_16x16x32_bf16 v[84:87], a[92:95], a[44:47], v[84:87]\n\t"
      "v_exp_f32 v78, v78\n\t"
      "v_mfma_f32_16x16x32_bf16 v[88:91], a[64:67], a[48:51], 0\n\t"
      "ds_read_b128 a[64:67], v40 offset:0\n\t"
      "v_fma_f32 v81, v81, s62, v38\n\t"
      "v_mfma_f32_16x16x32_bf16 v[88:91], a[68:71], a[52:55], v[88:91]\n\t"
      "v_exp_f32 v79, v79\n\t"
      "v_mfma_f32_16x16x32_bf16 v[88:91], a[72:75], a[56:59], v[88:91]\n\t"
      "ds_read_b128 a[68:71], v40 offset:256\n\t"
      "v_cvt_pk_bf16_f32 v103, v78, v79\n\t"
      "v_mfma_f32_16x16x32_bf16 v[88:91], a[76:79], a[60:63], v[88:91]\n\t"
      "v_exp_f32 v80, v80\n\t"
      "v_mfma_f32_16x16x32_bf16 v[92:95], a[80:83], a[48:51], 0\n\t"
      "ds_read_b128 a[72:75], v40 offset:512\n\t"
      "buffer_load_dwordx4 %[vv0], s[40:43], s55 offen offset:2048 lds\n\t"
      "v_mfma_f32_16x16x32_bf16 v[92:95], a[84:87], a[52:55], v[92:95]\n\t"
      "v_exp_f32 v81, v81\n\t"
      "v_mfma_f32_16x16x32_bf16 v[92:95], a[88:91], a[56:59], v[92:95]\n\t"
      "ds_read_b128 a[76:79], v40 offset:768\n\t"
      "v_cvt_pk_bf16_f32 v104, v80, v81\n\t"
      "v_mfma_f32_16x16x32_bf16 v[92:95], a[92:95], a[60:63], v[92:95]\n\t"
      "s_waitcnt lgkmcnt(5)\n\t"
      "v_fma_f32 v82, v82, s62, v38\n\t"
      "v_fma_f32 v83, v83, s62, v38\n\t"
      "v_mfma_f32_16x16x32_bf16 v[128:131], a[96:99], v[96:99], v[128:131]\n\t"
      "ds_read_b128 a[80:83], v40 offset:8192\n\t"
      "v_fma_f32 v84, v84, s62, v38\n\t"
      "v_mfma_f32_16x16x32_bf16 v[132:135], a[100:103], v[96:99], v[132:135]\n\t"
      "v_exp_f32 v82, v82\n\t"
      "v_mfma_f32_16x16x32_bf16 v[136:139], a[104:107], v[96:99], v[136:139]\n\t"
      "ds_read_b128 a[84:87], v40 offset:8448\n\t"
      "v_fma_f32 v85, v85, s62, v38\n\t"
      "v_mfma_f32_16x16x32_bf16 v[140:143], a[108:111], v[96:99], v[140:143]\n\t"
      "v_exp_f32 v83, v83\n\t"
      "v_mfma_f32_16x16x32_bf16 v[144:147], a[112:115], v[96:99], v[144:147]\n\t"
      "ds_read_b128 a[88:91], v40 offset:8704\n\t"
      "v_cvt_pk_bf16_f32 v105, v82, v83\n\t"
      "v_mfma_f32_16x16x32_bf16 v[148:151], a[116:119], v[96:99], v[148:151]\n\t"
      "v_exp_f32 v84, v84\n\t"
      "v_mfma_f32_16x16x32_bf16 v[152:155], a[120:123], v[96:99], v[152:155]\n\t"
      "ds_read_b128 a[92:95], v40 offset:8960\n\t"
      "buffer_load_dwordx4 %[vv1], s[40:43], s55 offen offset:3072 lds\n\t"
      "s_waitcnt lgkmcnt(5)\n\t"
      "v_mfma_f32_16x16x32_bf16 v[156:159], a[124:127], v[96:99], v[156:159]\n\t"
      "s_add_u32 s55, s55, s86\n\t"
      "v_exp_f32 v85, v85\n\t"
      "v_mfma_f32_16x16x32_bf16 a[132:135], a[128:131], v[96:99], a[132:135]\n\t"
      "v_cvt_pk_bf16_f32 v106, v84, v85\n\t"
      "v_fma_f32 v86, v86, s62, v38\n\t"
      "v_mfma_f32_16x16x32_bf16 v[160:163], a[96:99], v[100:103], v[160:163]\n\t"
      "v_fma_f32 v87, v87, s62, v38\n\t"
      "v_fma_f32 v88, v88, s62, v39\n\t"
      "v_mfma_f32_16x16x32_bf16 v[164:167], a[100:103], v[100:103], v[164:167]\n\t"
      "s_sub_u32 s58, s58, 1\n\t"
      "v_exp_f32 v86, v86\n\t"
      "v_mfma_f32_16x16x32_bf16 v[168:171], a[104:107], v[100:103], v[168:171]\n\t"
      "v_exp_f32 v87, v87\n\t"
      "v_mfma_f32_16x16x32_bf16 v[172:175], a[108:111], v[100:103], v[172:175]\n\t"
      "v_cvt_pk_bf16_f32 v107, v86, v87\n\t"
      "v_fma_f32 v89, v89, s62, v39\n\t"
      "v_mfma_f32_16x16x32_bf16 v[176:179], a[112:115], v[100:103], v[176:179]\n\t"
      "v_exp_f32 v88, v88\n\t"
      "v_mfma_f32_16x16x32_bf16 v[180:183], a[116:119], v[100:103], v[180:183]\n\t"
      "v_exp_f32 v89, v89\n\t"
      "v_mfma_f32_16x16x32_bf16 v[184:187], a[120:123], v[100:103], v[184:187]\n\t"
      "v_cvt_pk_bf16_f32 v108, v88, v89\n\t"
      "v_fma_f32 v90, v90, s62, v39\n\t"
      "v_mfma_f32_16x16x32_bf16 v[188:191], a[124:127], v[100:103], v[188:191]\n\t"
      "v_fma_f32 v91, v91, s62, v39\n\t"
      "v_exp_f32 v90, v90\n\t"
      "v_exp_f32 v91, v91\n\t"
      "s_nop 0\n\t"
      "v_cvt_pk_bf16_f32 v109, v90, v91\n\t"
      "v_mfma_f32_16x16x32_bf16 a[136:139], a[128:131], v[100:103], a[136:139]\n\t"
      "s_cbranch_scc0 Lld%=\n\t"
      "s_sub_u32 s56, s56, 1\n\t"
      "s_cbranch_scc1 Lps7_%=\n\t"
      "Lpr7_%=:\n\t"
      "s_cmp_lg_u32 s60, 0\n\t"
      "s_cbranch_scc1 Led%=\n\t"
      "Lpd%=:\n\t"
      "s_sub_u32 s58, s89, 1\n\t"
      "s_cbranch_scc1 Ltd%=\n\t"
      "s_mov_b32 s89, 0\n\t"
      "Lqd%=:\n\t"
      "s_waitcnt lgkmcnt(0)\n\t"
      "v_mfma_f32_16x16x32_bf16 v[64:67], a[64:67], a[0:3], 0\n\t"
      "s_add_u32 m0, s91, 0\n\t"
      "v_fma_f32 v92, v92, s62, v39\n\t"
      "v_fma_f32 v93, v93, s62, v39\n\t"
      "v_mfma_f32_16x16x32_bf16 v[64:67], a[68:71], a[4:7], v[64:67]\n\t"
      "buffer_load_dwordx4 %[vqa], s[44:47], s92 offen offset:0  lds\n\t"
      "v_fma_f32 v94, v94, s62, v39\n\t"
      "v_mfma_f32_16x16x32_bf16 v[64:67], a[72:75], a[8:11], v[64:67]\n\t"
      "s_add_u32 s92, s92, 15360\n\t"
      "buffer_load_dwordx4 %[vqa], s[44:47], s92 offen offset:1024  lds\n\t"
      "v_mfma_f32_16x16x32_bf16 v[64:67], a[76:79], a[12:15], v[64:67]\n\t"
      "s_add_u32 s92, s92, 15360\n\t"
      "v_exp_f32 v92, v92\n\t"
      "v_mfma_f32_16x16x32_bf16 v[68:71], a[80:83], a[0:3], 0\n\t"
      "v_exp_f32 v93, v93\n\t"
      "v_mfma_f32_16x16x32_bf16 v[68:71], a[84:87], a[4:7], v[68:71]\n\t"
      "buffer_load_dwordx4 %[vqb], s[44:47], s92 offen offset:2048  lds\n\t"
      "v_cvt_pk_bf16_f32 v110, v92, v93\n\t"
      "v_mfma_f32_16x16x32_bf16 v[68:71], a[88:91], a[8:11], v[68:71]\n\t"
      "s_add_u32 s92, s92, 15360\n\t"
      "v_fma_f32 v95, v95, s62, v39\n\t"
      "v_fma_f32 v64, v64, s62, v36\n\t"
      "v_mfma_f32_16x16x32_bf16 v[68:71], a[92:95], a[12:15], v[68:71]\n\t"
      "v_exp_f32 v94, v94\n\t"
      "v_mfma_f32_16x16x32_bf16 v[72:75], a[64:67], a[16:19], 0\n\t"
      "buffer_load_dwordx4 %[vqb], s[44:47], s92 offen offset:3072  lds\n\t"
      "v_fma_f32 v65, v65, s62, v36\n\t"
      "v_mfma_f32_16x16x32_bf16 v[72:75], a[68:71], a[20:23], v[72:75]\n\t"
      "s_add_u32 s92, s92, 19456\n\t"
      "v_exp_f32 v95, v95\n\t"
      "v_mfma_f32_16x16x32_bf16 v[72:75], a[72:75], a[24:27], v[72:75]\n\t"
      "s_add_u32 m0, s91, 4096\n\t"
      "v_cvt_pk_bf16_f32 v111, v94, v95\n\t"
      "v_fma_f32 v66, v66, s62, v36\n\t"
      "v_mfma_f32_16x16x32_bf16 v[72:75], a[76:79], a[28:31], v[72:75]\n\t"
      "buffer_load_dwordx4 %[vqa], s[44:47], s92 offen offset:0  lds\n\t"
      "v_fma_f32 v67, v67, s62, v36\n\t"
      "v_mfma_f32_16x16x32_bf16 v[76:79], a[80:83], a[16:19], 0\n\t"
      "s_add_u32 s92, s92, 15360\n\t"
      "v_exp_f32 v64, v64\n\t"
      "v_mfma_f32_16x16x32_bf16 v[76:79], a[84:87], a[20:23], v[76:79]\n\t"
      "v_exp_f32 v65, v65\n\t"
      "v_mfma_f32_16x16x32_bf16 v[76:79], a[88:91], a[24:27], v[76:79]\n\t"
      "buffer_load_dwordx4 %[vqa], s[44:47], s92 offen offset:1024  lds\n\t"
      "v_cvt_pk_bf16_f32 v96, v64, v65\n\t"
      "v_mfma_f32_16x16x32_bf16 v[76:79], a[92:95], a[28:31], v[76:79]\n\t"
      "s_add_u32 s92, s92, 15360\n\t"
      "v_exp_f32 v66, v66\n\t"
      "v_mfma_f32_16x16x32_bf16 v[192:195], a[96:99], v[104:107], v[192:195]\n\t"
      "v_exp_f32 v67, v67\n\t"
      "v_mfma_f32_16x16x32_bf16 v[196:199], a[100:103], v[104:107], v[196:199]\n\t"
      "buffer_load_dwordx4 %[vqb], s[44:47], s92 offen offset:2048  lds\n\t"
      "v_cvt_pk_bf16_f32 v97, v66, v67\n\t"
      "v_mfma_f32_16x16x32_bf16 v[200:203], a[104:107], v[104:107], v[200:203]\n\t"
      "s_add_u32 s92, s92, 15360\n\t"
      "v_fma_f32 v68, v68, s62, v36\n\t"
      "v_fma_f32 v69, v69, s62, v36\n\t"
      "v_mfma_f32_16x16x32_bf16 v[204:207], a[108:111], v[104:107], v[204:207]\n\t"
      "buffer_load_dwordx4 %[vqb], s[44:47], s92 offen offset:3072  lds\n\t"
      "v_fma_f32 v70, v70, s62, v36\n\t"
      "v_mfma_f32_16x16x32_bf16 v[208:211], a[112:115], v[104:107], v[208:211]\n\t"
      "s_sub_u32 s92, s92, 242688\n\t"
      "v_exp_f32 v68, v68\n\t"
      "v_mfma_f32_16x16x32_bf16 v[212:215], a[116:119], v[104:107], v[212:215]\n\t"
      "s_waitcnt vmcnt(20)\n\t"
      "v_exp_f32 v69, v69\n\t"
      "v_mfma_f32_16x16x32_bf16 v[216:219], a[120:123], v[104:107], v[216:219]\n\t"
      "s_barrier\n\t"
      "v_cvt_pk_bf16_f32 v98, v68, v69\n\t"
      "v_fma_f32 v71, v71, s62, v36\n\t"
      "v_mfma_f32_16x16x32_bf16 v[220:223], a[124:127], v[104:107], v[220:223]\n\t"
      "s_sub_u32 s91, s91, 8192\n\t"
      "v_exp_f32 v70, v70\n\t"
      "v_mfma_f32_16x16x32_bf16 a[140:143], a[128:131], v[104:107], a[140:143]\n\t"
      "s_add_u32 m0, s52, s54\n\t"
      "v_exp_f32 v71, v71\n\t"
      "v_mfma_f32_16x16x32_bf16 v[224:227], a[96:99], v[108:111], v[224:227]\n\t"
      "s_mov_b32 s52, s53\n\t"
      "v_add_u32 v41, s52, %[vb0]\n\t"
      "v_add_u32 v42, s52, %[vb1]\n\t"
      "v_mfma_f32_16x16x32_bf16 v[228:231], a[100:103], v[108:111], v[228:231]\n\t"
      "s_add_u32 s53, s53, 16384\n\t"
      "ds_read_b64_tr_b16 a[96:97], v41 offset:0\n\t"
      "v_mfma_f32_16x16x32_bf16 v[232:235], a[104:107], v[108:111], v[232:235]\n\t"
      "s_cmp_eq_u32 s53, 98304\n\t"
      "ds_read_b64_tr_b16 a[98:99], v41 offset:8192\n\t"
      "v_mfma_f32_16x16x32_bf16 v[236:239], a[108:111], v[108:111], v[236:239]\n\t"
      "s_cselect_b32 s53, 0, s53\n\t"
      "v_add_u32 v40, s53, %[kb]\n\t"
      "v_cvt_pk_bf16_f32 v99, v70, v71\n\t"
      "v_mfma_f32_16x16x32_bf16 v[240:243], a[112:115], v[108:111], v[240:243]\n\t"
      "ds_read_b64_tr_b16 a[100:101], v42 offset:0\n\t"
      "ds_read_b64_tr_b16 a[102:103], v42 offset:8192\n\t"
      "v_mfma_f32_16x16x32_bf16 v[244:247], a[116:119], v[108:111], v[244:247]\n\t"
      "ds_read_b64_tr_b16 a[104:105], v41 offset:512\n\t"
      "ds_read_b64_tr_b16 a[106:107], v41 offset:8704\n\t"
      "v_mfma_f32_16x16x32_bf16 v[248:251], a[120:123], v[108:111], v[248:251]\n\t"
      "ds_read_b64_tr_b16 a[108:109], v42 offset:512\n\t"
      "ds_read_b64_tr_b16 a[110:111], v42 offset:8704\n\t"
      "v_mfma_f32_16x16x32_bf16 v[252:255], a[124:127], v[108:111], v[252:255]\n\t"
      "ds_read_b64_tr_b16 a[112:113], v41 offset:1024\n\t"
      "ds_read_b64_tr_b16 a[114:115], v41 offset:9216\n\t"
      "v_mfma_f32_16x16x32_bf16 a[144:147], a[128:131], v[108:111], a[144:147]\n\t"
      "ds_read_b64_tr_b16 a[116:117], v42 offset:1024\n\t"
      "ds_read_b64_tr_b16 a[118:119], v42 offset:9216\n\t"
      "v_mfma_f32_16x16x32_bf16 v[80:83], a[64:67], a[32:35], 0\n\t"
      "ds_read_b64_tr_b16 a[120:121], v41 offset:1536\n\t"
      "ds_read_b64_tr_b16 a[122:123], v41 offset:9728\n\t"
      "v_mfma_f32_16x16x32_bf16 v[80:83], a[68:71], a[36:39], v[80:83]\n\t"
      "ds_read_b64_tr_b16 a[124:125], v42 offset:1536\n\t"
      "ds_read_b64_tr_b16 a[126:127], v42 offset:9728\n\t"
      "v_mfma_f32_16x16x32_bf16 v[80:83], a[72:75], a[40:43], v[80:83]\n\t"
      "buffer_load_dwordx4 %[vk0], s[36:39], s55 offen lds\n\t"
      "buffer_load_dwordx4 %[vk1], s[36:39], s55 offen offset:1024 lds\n\t"
      "v_mfma_f32_16x16x32_bf16 v[80:83], a[76:79], a[44:47], v[80:83]\n\t"
      "v_fma_f32 v72, v72, s62, v37\n\t"
      "v_fma_f32 v73, v73, s62, v37\n\t"
      "v_mfma_f32_16x16x32_bf16 v[84:87], a[80:83], a[32:35], 0\n\t"
      "v_exp_f32 v72, v72\n\t"
      "v_mfma_f32_16x16x32_bf16 v[84:87], a[84:87], a[36:39], v[84:87]\n\t"
      "v_exp_f32 v73, v73\n\t"
      "v_mfma_f32_16x16x32_bf16 v[84:87], a[88:91], a[40:43], v[84:87]\n\t"
      "v_cvt_pk_bf16_f32 v100, v72, v73\n\t"
      "v_fma_f32 v74, v74, s62, v37\n\t"
      "v_mfma_f32_16x16x32_bf16 v[84:87], a[92:95], a[44:47], v[84:87]\n\t"
      "v_fma_f32 v75, v75, s62, v37\n\t"
      "v_fma_f32 v76, v76, s62, v37\n\t"
      "v_mfma_f32_16x16x32_bf16 v[88:91], a[64:67], a[48:51], 0\n\t"
      "ds_read_b128 a[64:67], v40 offset:0\n\t"
      "v_fma_f32 v77, v77, s62, v37\n\t"
      "v_mfma_f32_16x16x32_bf16 v[88:91], a[68:71], a[52:55], v[88:91]\n\t"
      "ds_read_b128 a[68:71], v40 offset:256\n\t"
      "v_fma_f32 v78, v78, s62, v37\n\t"
      "v_mfma_f32_16x16x32_bf16 v[88:91], a[72:75], a[56:59], v[88:91]\n\t"
      "ds_read_b128 a[72:75], v40 offset:512\n\t"
      "v_fma_f32 v79, v79, s62, v37\n\t"
      "v_mfma_f32_16x16x32_bf16 v[88:91], a[76:79], a[60:63], v[88:91]\n\t"
      "ds_read_b128 a[76:79], v40 offset:768\n\t"
      "v_fma_f32 v80, v80, s62, v38\n\t"
      "v_mfma_f32_16x16x32_bf16 v[92:95], a[80:83], a[48:51], 0\n\t"
      "ds_read_b128 a[80:83], v40 offset:8192\n\t"
      "buffer_load_dwordx4 %[vv0], s[40:43], s55 offen offset:2048 lds\n\t"
      "v_mfma_f32_16x16x32_bf16 v[92:95], a[84:87], a[52:55], v[92:95]\n\t"
      "ds_read_b128 a[84:87], v40 offset:8448\n\t"
      "v_fma_f32 v81, v81, s62, v38\n\t"
      "v_mfma_f32_16x16x32_bf16 v[92:95], a[88:91], a[56:59], v[92:95]\n\t"
      "ds_read_b128 a[88:91], v40 offset:8704\n\t"
      "v_fma_f32 v82, v82, s62, v38\n\t"
      "v_mfma_f32_16x16x32_bf16 v[92:95], a[92:95], a[60:63], v[92:95]\n\t"
      "ds_read_b128 a[92:95], v40 offset:8960\n\t"
      "v_fma_f32 v83, v83, s62, v38\n\t"
      "s_waitcnt lgkmcnt(8)\n\t"
      "v_mfma_f32_16x16x32_bf16 v[128:131], a[96:99], v[96:99], v[128:131]\n\t"
      "v_exp_f32 v74, v74\n\t"
      "v_mfma_f32_16x16x32_bf16 v[132:135], a[100:103], v[96:99], v[132:135]\n\t"
      "v_exp_f32 v75, v75\n\t"
      "v_mfma_f32_16x16x32_bf16 v[136:139], a[104:107], v[96:99], v[136:139]\n\t"
      "v_cvt_pk_bf16_f32 v101, v74, v75\n\t"
      "v_fma_f32 v84, v84, s62, v38\n\t"
      "v_mfma_f32_16x16x32_bf16 v[140:143], a[108:111], v[96:99], v[140:143]\n\t"
      "v_exp_f32 v76, v76\n\t"
      "v_mfma_f32_16x16x32_bf16 v[144:147], a[112:115], v[96:99], v[144:147]\n\t"
      "v_exp_f32 v77, v77\n\t"
      "v_mfma_f32_16x16x32_bf16 v[148:151], a[116:119], v[96:99], v[148:151]\n\t"
      "v_cvt_pk_bf16_f32 v102, v76, v77\n\t"
      "v_fma_f32 v85, v85, s62, v38\n\t"
      "v_mfma_f32_16x16x32_bf16 v[152:155], a[120:123], v[96:99], v[152:155]\n\t"
      "buffer_load_dwordx4 %[vv1], s[40:43], s55 offen offset:3072 lds\n\t"
      "v_fma_f32 v86, v86, s62, v38\n\t"
      "v_mfma_f32_16x16x32_bf16 v[156:159], a[124:127], v[96:99], v[156:159]\n\t"
      "s_add_u32 s55, s55, s86\n\t"
      "v_exp_f32 v78, v78\n\t"
      "v_mfma_f32_16x16x32_bf16 a[132:135], a[128:131], v[96:99], a[132:135]\n\t"
      "s_sub_u32 s56, s56, 1\n\t"
      "s_cbranch_scc1 Lps9_%=\n\t"
      "Lpr9_%=:\n\t"
      "v_exp_f32 v79, v79\n\t"
      "v_exp_f32 v80, v80\n\t"
      "v_cvt_pk_bf16_f32 v103, v78, v79\n\t"
      "v_exp_f32 v81, v81\n\t"
      "s_nop 0\n\t"
      "v_mfma_f32_16x16x32_bf16 v[160:163], a[96:99], v[100:103], v[160:163]\n\t"
      "v_cvt_pk_bf16_f32 v104, v80, v81\n\t"
      "v_fma_f32 v87, v87, s62, v38\n\t"
      "v_mfma_f32_16x16x32_bf16 v[164:167], a[100:103], v[100:103], v[164:167]\n\t"
      "s_sub_u32 s58, s58, 1\n\t"
      "v_exp_f32 v82, v82\n\t"
      "v_mfma_f32_16x16x32_bf16 v[168:171], a[104:107], v[100:103], v[168:171]\n\t"
      "v_exp_f32 v83, v83\n\t"
      "v_mfma_f32_16x16x32_bf16 v[172:175], a[108:111], v[100:103], v[172:175]\n\t"
      "v_cvt_pk_bf16_f32 v105, v82, v83\n\t"
      "v_fma_f32 v88, v88, s62, v39\n\t"
      "v_mfma_f32_16x16x32_bf16 v[176:179], a[112:115], v[100:103], v[176:179]\n\t"
      "v_exp_f32 v84, v84\n\t"
      "v_mfma_f32_16x16x32_bf16 v[180:183], a[116:119], v[100:103], v[180:183]\n\t"
      "v_exp_f32 v85, v85\n\t"
      "v_mfma_f32_16x16x32_bf16 v[184:187], a[120:123], v[100:103], v[184:187]\n\t"
      "v_cvt_pk_bf16_f32 v106, v84, v85\n\t"
      "v_fma_f32 v89, v89, s62, v39\n\t"
      "v_mfma_f32_16x16x32_bf16 v[188:191], a[124:127], v[100:103], v[188:191]\n\t"
      "v_exp_f32 v86, v86\n\t"
      "v_exp_f32 v87, v87\n\t"
      "v_exp_f32 v88, v88\n\t"
      "v_cvt_pk_bf16_f32 v107, v86, v87\n\t"
      "v_exp_f32 v89, v89\n\t"
      "v_fma_f32 v90, v90, s62, v39\n\t"
      "v_cvt_pk_bf16_f32 v108, v88, v89\n\t"
      "v_fma_f32 v91, v91, s62, v39\n\t"
      "v_exp_f32 v90, v90\n\t"
      "v_exp_f32 v91, v91\n\t"
      "s_nop 0\n\t"
      "v_cvt_pk_bf16_f32 v109, v90, v91\n\t"
      "v_mfma_f32_16x16x32_bf16 a[136:139], a[128:131], v[100:103], a[136:139]\n\t"
      "s_cbranch_scc0 Lqd%=\n\t"
      "s_mov_b32 s60, s90\n\t"
      "s_cmp_lg_u32 s90, 0\n\t"
      "s_cbranch_scc1 Led%=\n\t"
      "Ltd%=:\n\t"
      "s_waitcnt vmcnt(4)\n\t"
      "s_mov_b64 s[72:73], 0\n\t"
      "v_mfma_f32_16x16x32_bf16 v[192:195], a[96:99], v[104:107], v[192:195]\n\t"
      "ds_read_b128 a[32:35], %[qb] offset:8192\n\t"
      "v_fma_f32 v92, v92, s62, v39\n\t"
      "v_mfma_f32_16x16x32_bf16 v[196:199], a[100:103], v[104:107], v[196:199]\n\t"
      "ds_read_b128 a[36:39], %[qb] offset:8448\n\t"
      "v_fma_f32 v93, v93, s62, v39\n\t"
      "v_mfma_f32_16x16x32_bf16 v[200:203], a[104:107], v[104:107], v[200:203]\n\t"
      "ds_read_b128 a[40:43], %[qb] offset:8704\n\t"
      "v_fma_f32 v94, v94, s62, v39\n\t"
      "v_mfma_f32_16x16x32_bf16 v[204:207], a[108:111], v[104:107], v[204:207]\n\t"
      "ds_read_b128 a[44:47], %[qb] offset:8960\n\t"
      "v_fma_f32 v95, v95, s62, v39\n\t"
      "v_mfma_f32_16x16x32_bf16 v[208:211], a[112:115], v[104:107], v[208:211]\n\t"
      "ds_read_b128 a[48:51], %[qb] offset:12288\n\t"
      "v_accvgpr_read_b32 v48, a132\n\t"
      "v_mfma_f32_16x16x32_bf16 v[212:215], a[116:119], v[104:107], v[212:215]\n\t"
      "ds_read_b128 a[52:55], %[qb] offset:12544\n\t"
      "v_cmp_ngt_f32 vcc, s63, v48\n\t"
      "v_mfma_f32_16x16x32_bf16 v[216:219], a[120:123], v[104:107], v[216:219]\n\t"
      "ds_read_b128 a[56:59], %[qb] offset:12800\n\t"
      "s_or_b64 s[72:73], s[72:73], vcc\n\t"
      "s_sub_u32 s68, s61, 131072\n\t"
      "v_mfma_f32_16x16x32_bf16 v[220:223], a[124:127], v[104:107], v[220:223]\n\t"
      "ds_read_b128 a[60:63], %[qb] offset:13056\n\t"
      "v_accvgpr_read_b32 v50, a136\n\t"
      "v_mfma_f32_16x16x32_bf16 a[140:143], a[128:131], v[104:107], a[140:143]\n\t"
      "v_exp_f32 v92, v92\n\t"
      "v_exp_f32 v93, v93\n\t"
      "v_exp_f32 v94, v94\n\t"
      "v_cvt_pk_bf16_f32 v110, v92, v93\n\t"
      "v_exp_f32 v95, v95\n\t"
      "v_rcp_f32 v48, v48\n\t"
      "v_cvt_pk_bf16_f32 v111, v94, v95\n\t"
      "v_mul_f32 v128, v48, v128\n\t"
      "v_mul_f32 v129, v48, v129\n\t"
      "v_mfma_f32_16x16x32_bf16 v[224:227], a[96:99], v[108:111], v[224:227]\n\t"
      "v_mul_f32 v130, v48, v130\n\t"
      "v_mul_f32 v131, v48, v131\n\t"
      "v_mfma_f32_16x16x32_bf16 v[228:231], a[100:103], v[108:111], v[228:231]\n\t"
      "v_cvt_pk_bf16_f32 v128, v128, v129\n\t"
      "v_cvt_pk_bf16_f32 v129, v130, v131\n\t"
      "v_mfma_f32_16x16x32_bf16 v[232:235], a[104:107], v[108:111], v[232:235]\n\t"
      "ds_write_b64 v112, v[128:129] offset:8192\n\t"
      "v_mfma_f32_16x16x32_bf16 v[236:239], a[108:111], v[108:111], v[236:239]\n\t"
      "v_mul_f32 v132, v48, v132\n\t"
      "v_mul_f32 v133, v48, v133\n\t"
      "v_mfma_f32_16x16x32_bf16 v[240:243], a[112:115], v[108:111], v[240:243]\n\t"
      "v_mul_f32 v134, v48, v134\n\t"
      "v_mul_f32 v135, v48, v135\n\t"
      "v_mfma_f32_16x16x32_bf16 v[244:247], a[116:119], v[108:111], v[244:247]\n\t"
      "v_cvt_pk_bf16_f32 v130, v132, v133\n\t"
      "v_cvt_pk_bf16_f32 v131, v134, v135\n\t"
      "v_mfma_f32_16x16x32_bf16 v[248:251], a[120:123], v[108:111], v[248:251]\n\t"
      "ds_write_b64 v113, v[130:131] offset:8192\n\t"
      "v_mfma_f32_16x16x32_bf16 v[252:255], a[124:127], v[108:111], v[252:255]\n\t"
      "v_mul_f32 v136, v48, v136\n\t"
      "v_mul_f32 v137, v48, v137\n\t"
      "v_mfma_f32_16x16x32_bf16 a[144:147], a[128:131], v[108:111], a[144:147]\n\t"
      "v_mul_f32 v138, v48, v138\n\t"
      "v_mul_f32 v139, v48, v139\n\t"
      "v_cvt_pk_bf16_f32 v136, v136, v137\n\t"
      "v_cvt_pk_bf16_f32 v137, v138, v139\n\t"
      "ds_write_b64 v114, v[136:137] offset:8192\n\t"
      "v_mul_f32 v140, v48, v140\n\t"
      "v_mul_f32 v141, v48, v141\n\t"
      "v_mul_f32 v142, v48, v142\n\t"
      "v_mul_f32 v143, v48, v143\n\t"
      "v_cvt_pk_bf16_f32 v138, v140, v141\n\t"
      "v_cvt_pk_bf16_f32 v139, v142, v143\n\t"
      "ds_write_b64 v115, v[138:139] offset:8192\n\t"
      "v_mul_f32 v144, v48, v144\n\t"
      "v_mul_f32 v145, v48, v145\n\t"
      "v_mul_f32 v146, v48, v146\n\t"
      "v_mul_f32 v147, v48, v147\n\t"
      "v_cvt_pk_bf16_f32 v144, v144, v145\n\t"
      "v_cvt_pk_bf16_f32 v145, v146, v147\n\t"
      "ds_write_b64 v116, v[144:145] offset:8192\n\t"
      "v_mul_f32 v148, v48, v148\n\t"
      "v_mul_f32 v149, v48, v149\n\t"
      "v_mul_f32 v150, v48, v150\n\t"
      "v_mul_f32 v151, v48, v151\n\t"
      "v_cvt_pk_bf16_f32 v146, v148, v149\n\t"
      "v_cvt_pk_bf16_f32 v147, v150, v151\n\t"
      "ds_write_b64 v117, v[146:147] offset:8192\n\t"
      "v_mul_f32 v152, v48, v152\n\t"
      "v_mul_f32 v153, v48, v153\n\t"
      "v_mul_f32 v154, v48, v154\n\t"
      "v_mul_f32 v155, v48, v155\n\t"
      "v_cvt_pk_bf16_f32 v152, v152, v153\n\t"
      "v_cvt_pk_bf16_f32 v153, v154, v155\n\t"
      "ds_write_b64 v118, v[152:153] offset:8192\n\t"
      "v_mul_f32 v156, v48, v156\n\t"
      "v_mul_f32 v157, v48, v157\n\t"
      "v_mul_f32 v158, v48, v158\n\t"
      "v_mul_f32 v159, v48, v159\n\t"
      "v_cvt_pk_bf16_f32 v154, v156, v157\n\t"
      "v_cvt_pk_bf16_f32 v155, v158, v159\n\t"
      "ds_write_b64 v119, v[154:155] offset:8192\n\t"
      "ds_read_b128 v[128:131], v120 offset:8192\n\t"
      "v_cmp_ngt_f32 vcc, s63, v50\n\t"
      "ds_read_b128 v[136:139], v121 offset:8192\n\t"
      "s_or_b64 s[72:73], s[72:73], vcc\n\t"
      "v_rcp_f32 v50, v50\n\t"
      "ds_read_b128 v[144:147], v122 offset:8192\n\t"
      "s_sub_u32 s69, s61, 65536\n\t"
      "v_mov_b32 v51, v50\n\t"
      "ds_read_b128 v[152:155], v123 offset:8192\n\t"
      "v_pk_mul_f32 v[160:161], v[160:161], v[50:51]\n\t"
      "v_pk_mul_f32 v[162:163], v[162:163], v[50:51]\n\t"
      "v_cvt_pk_bf16_f32 v160, v160, v161\n\t"
      "v_cvt_pk_bf16_f32 v161, v162, v163\n\t"
      "ds_write_b64 v112, v[160:161] offset:12288\n\t"
      "v_pk_mul_f32 v[164:165], v[164:165], v[50:51]\n\t"
      "v_pk_mul_f32 v[166:167], v[166:167], v[50:51]\n\t"
      "v_cvt_pk_bf16_f32 v162, v164, v165\n\t"
      "v_cvt_pk_bf16_f32 v163, v166, v167\n\t"
      "ds_write_b64 v113, v[162:163] offset:12288\n\t"
      "s_waitcnt lgkmcnt(5)\n\t"
      "buffer_store_dwordx4 v[128:131], v53, s[48:51], s68 offen sc1 nt\n\t"
      "v_pk_mul_f32 v[168:169], v[168:169], v[50:51]\n\t"
      "v_pk_mul_f32 v[170:171], v[170:171], v[50:51]\n\t"
      "v_cvt_pk_bf16_f32 v168, v168, v169\n\t"
      "v_cvt_pk_bf16_f32 v169, v170, v171\n\t"
      "ds_write_b64 v114, v[168:169] offset:12288\n\t"
      "v_pk_mul_f32 v[172:173], v[172:173], v[50:51]\n\t"
      "v_pk_mul_f32 v[174:175], v[174:175], v[50:51]\n\t"
      "v_cvt_pk_bf16_f32 v170, v172, v173\n\t"
      "v_cvt_pk_bf16_f32 v171, v174, v175\n\t"
      "ds_write_b64 v115, v[170:171] offset:12288\n\t"
      "s_waitcnt lgkmcnt(6)\n\t"
      "buffer_store_dwordx4 v[136:139], v54, s[48:51], s68 offen sc1 nt\n\t"
      "v_pk_mul_f32 v[176:177], v[176:177], v[50:51]\n\t"
      "v_pk_mul_f32 v[178:179], v[178:179], v[50:51]\n\t"
      "v_cvt_pk_bf16_f32 v176, v176, v177\n\t"
      "v_cvt_pk_bf16_f32 v177, v178, v179\n\t"
      "ds_write_b64 v116, v[176:177] offset:12288\n\t"
      "v_pk_mul_f32 v[180:181], v[180:181], v[50:51]\n\t"
      "v_pk_mul_f32 v[182:183], v[182:183], v[50:51]\n\t"
      "v_cvt_pk_bf16_f32 v178, v180, v181\n\t"
      "v_cvt_pk_bf16_f32 v179, v182, v183\n\t"
      "ds_write_b64 v117, v[178:179] offset:12288\n\t"
      "s_waitcnt lgkmcnt(7)\n\t"
      "buffer_store_dwordx4 v[144:147], v55, s[48:51], s68 offen sc1 nt\n\t"
      "v_pk_mul_f32 v[184:185], v[184:185], v[50:51]\n\t"
      "v_pk_mul_f32 v[186:187], v[186:187], v[50:51]\n\t"
      "v_cvt_pk_bf16_f32 v184, v184, v185\n\t"
      "v_cvt_pk_bf16_f32 v185, v186, v187\n\t"
      "ds_write_b64 v118, v[184:185] offset:12288\n\t"
      "v_pk_mul_f32 v[188:189], v[188:189], v[50:51]\n\t"
      "v_pk_mul_f32 v[190:191], v[190:191], v[50:51]\n\t"
      "v_cvt_pk_bf16_f32 v186, v188, v189\n\t"
      "v_cvt_pk_bf16_f32 v187, v190, v191\n\t"
      "ds_write_b64 v119, v[186:187] offset:12288\n\t"
      "ds_read_b128 v[160:163], v120 offset:12288\n\t"
      "s_waitcnt lgkmcnt(9)\n\t"
      "buffer_store_dwordx4 v[152:155], v56, s[48:51], s68 offen sc1 nt\n\t"
      "ds_read_b128 v[168:171], v121 offset:12288\n\t"
      "v_accvgpr_read_b32 v48, a140\n\t"
      "ds_read_b128 v[176:179], v122 offset:12288\n\t"
      "v_cmp_ngt_f32 vcc, s63, v48\n\t"
      "ds_read_b128 v[184:187], v123 offset:12288\n\t"
      "s_or_b64 s[72:73], s[72:73], vcc\n\t"
      "v_rcp_f32 v48, v48\n\t"
      "v_accvgpr_read_b32 v50, a144\n\t"
      "v_mov_b32 v49, v48\n\t"
      "v_pk_mul_f32 v[192:193], v[192:193], v[48:49]\n\t"
      "v_pk_mul_f32 v[194:195], v[194:195], v[48:49]\n\t"
      "v_cvt_pk_bf16_f32 v132, v192, v193\n\t"
      "v_cvt_pk_bf16_f32 v133, v194, v195\n\t"
      "ds_write_b64 v112, v[132:133] offset:8192\n\t"
      "v_pk_mul_f32 v[196:197], v[196:197], v[48:49]\n\t"
      "v_pk_mul_f32 v[198:199], v[198:199], v[48:49]\n\t"
      "v_cvt_pk_bf16_f32 v134, v196, v197\n\t"
      "v_cvt_pk_bf16_f32 v135, v198, v199\n\t"
      "s_waitcnt lgkmcnt(4)\n\t"
      "buffer_store_dwordx4 v[160:163], v53, s[48:51], s69 offen sc1 nt\n\t"
      "ds_write_b64 v113, v[134:135] offset:8192\n\t"
      "v_pk_mul_f32 v[200:201], v[200:201], v[48:49]\n\t"
      "v_pk_mul_f32 v[202:203], v[202:203], v[48:49]\n\t"
      "v_cvt_pk_bf16_f32 v140, v200, v201\n\t"
      "v_cvt_pk_bf16_f32 v141, v202, v203\n\t"
      "ds_write_b64 v114, v[140:141] offset:8192\n\t"
      "v_pk_mul_f32 v[204:205], v[204:205], v[48:49]\n\t"
      "v_pk_mul_f32 v[206:207], v[206:207], v[48:49]\n\t"
      "v_cvt_pk_bf16_f32 v142, v204, v205\n\t"
      "v_cvt_pk_bf16_f32 v143, v206, v207\n\t"
      "ds_write_b64 v115, v[142:143] offset:8192\n\t"
      "s_waitcnt lgkmcnt(6)\n\t"
      "buffer_store_dwordx4 v[168:171], v54, s[48:51], s69 offen sc1 nt\n\t"
      "v_pk_mul_f32 v[208:209], v[208:209], v[48:49]\n\t"
      "v_pk_mul_f32 v[210:211], v[210:211], v[48:49]\n\t"
      "v_cvt_pk_bf16_f32 v148, v208, v209\n\t"
      "v_cvt_pk_bf16_f32 v149, v210, v211\n\t"
      "ds_write_b64 v116, v[148:149] offset:8192\n\t"
      "v_pk_mul_f32 v[212:213], v[212:213], v[48:49]\n\t"
      "v_pk_mul_f32 v[214:215], v[214:215], v[48:49]\n\t"
      "v_cvt_pk_bf16_f32 v150, v212, v213\n\t"
      "v_cvt_pk_bf16_f32 v151, v214, v215\n\t"
      "ds_write_b64 v117, v[150:151] offset:8192\n\t"
      "s_waitcnt lgkmcnt(7)\n\t"
      "buffer_store_dwordx4 v[176:179], v55, s[48:51], s69 offen sc1 nt\n\t"
      "v_pk_mul_f32 v[216:217], v[216:217], v[48:49]\n\t"
      "v_pk_mul_f32 v[218:219], v[218:219], v[48:49]\n\t"
      "v_cvt_pk_bf16_f32 v156, v216, v217\n\t"
      "v_cvt_pk_bf16_f32 v157, v218, v219\n\t"
      "ds_write_b64 v118, v[156:157] offset:8192\n\t"
      "v_pk_mul_f32 v[220:221], v[220:221], v[48:49]\n\t"
      "v_pk_mul_f32 v[222:223], v[222:223], v[48:49]\n\t"
      "v_cvt_pk_bf16_f32 v158, v220, v221\n\t"
      "v_cvt_pk_bf16_f32 v159, v222, v223\n\t"
      "ds_write_b64 v119, v[158:159] offset:8192\n\t"
      "ds_read_b128 v[132:135], v120 offset:8192\n\t"
      "s_waitcnt lgkmcnt(9)\n\t"
      "buffer_store_dwordx4 v[184:187], v56, s[48:51], s69 offen sc1 nt\n\t"
      "ds_read_b128 v[140:143], v121 offset:8192\n\t"
      "v_cmp_ngt_f32 vcc, s63, v50\n\t"
      "ds_read_b128 v[148:151], v122 offset:8192\n\t"
      "s_or_b64 s[72:73], s[72:73], vcc\n\t"
      "v_rcp_f32 v50, v50\n\t"
      "ds_read_b128 v[156:159], v123 offset:8192\n\t"
      "v_mov_b32 v51, v50\n\t"
      "v_pk_mul_f32 v[224:225], v[224:225], v[50:51]\n\t"
      "v_pk_mul_f32 v[226:227], v[226:227], v[50:51]\n\t"
      "v_cvt_pk_bf16_f32 v164, v224, v225\n\t"
      "v_cvt_pk_bf16_f32 v165, v226, v227\n\t"
      "ds_write_b64 v112, v[164:165] offset:12288\n\t"
      "v_pk_mul_f32 v[228:229], v[228:229], v[50:51]\n\t"
      "v_pk_mul_f32 v[230:231], v[230:231], v[50:51]\n\t"
      "v_cvt_pk_bf16_f32 v166, v228, v229\n\t"
      "v_cvt_pk_bf16_f32 v167, v230, v231\n\t"
      "ds_write_b64 v113, v[166:167] offset:12288\n\t"
      "v_pk_mul_f32 v[232:233], v[232:233], v[50:51]\n\t"
      "v_pk_mul_f32 v[234:235], v[234:235], v[50:51]\n\t"
      "v_cvt_pk_bf16_f32 v172, v232, v233\n\t"
      "v_cvt_pk_bf16_f32 v173, v234, v235\n\t"
      "ds_write_b64 v114, v[172:173] offset:12288\n\t"
      "v_pk_mul_f32 v[236:237], v[236:237], v[50:51]\n\t"
      "v_pk_mul_f32 v[238:239], v[238:239], v[50:51]\n\t"
      "v_cvt_pk_bf16_f32 v174, v236, v237\n\t"
      "v_cvt_pk_bf16_f32 v175, v238, v239\n\t"
      "ds_write_b64 v115, v[174:175] offset:12288\n\t"
      "v_pk_mul_f32 v[240:241], v[240:241], v[50:51]\n\t"
      "v_pk_mul_f32 v[242:243], v[242:243], v[50:51]\n\t"
      "v_cvt_pk_bf16_f32 v180, v240, v241\n\t"
      "v_cvt_pk_bf16_f32 v181, v242, v243\n\t"
      "ds_write_b64 v116, v[180:181] offset:12288\n\t"
      "v_pk_mul_f32 v[244:245], v[244:245], v[50:51]\n\t"
      "v_pk_mul_f32 v[246:247], v[246:247], v[50:51]\n\t"
      "v_cvt_pk_bf16_f32 v182, v244, v245\n\t"
      "v_cvt_pk_bf16_f32 v183, v246, v247\n\t"
      "ds_write_b64 v117, v[182:183] offset:12288\n\t"
      "v_pk_mul_f32 v[248:249], v[248:249], v[50:51]\n\t"
      "v_pk_mul_f32 v[250:251], v[250:251], v[50:51]\n\t"
      "v_cvt_pk_bf16_f32 v188, v248, v249\n\t"
      "v_cvt_pk_bf16_f32 v189, v250, v251\n\t"
      "ds_write_b64 v118, v[188:189] offset:12288\n\t"
      "v_pk_mul_f32 v[252:253], v[252:253], v[50:51]\n\t"
      "v_pk_mul_f32 v[254:255], v[254:255], v[50:51]\n\t"
      "v_cvt_pk_bf16_f32 v190, v252, v253\n\t"
      "v_cvt_pk_bf16_f32 v191, v254, v255\n\t"
      "ds_write_b64 v119, v[190:191] offset:12288\n\t"
      "ds_read_b128 v[164:167], v120 offset:12288\n\t"
      "ds_read_b128 v[172:175], v121 offset:12288\n\t"
      "ds_read_b128 v[180:183], v122 offset:12288\n\t"
      "ds_read_b128 v[188:191], v123 offset:12288\n\t"
      "s_cmp_lg_u64 s[72:73], 0\n\t"
      "s_cselect_b32 s76, 1, 0\n\t"
      "s_mov_b32 s77, 0\n\t"
      "s_lshl_b64 s[76:77], s[76:77], s59\n\t"
      "s_or_b64 s[74:75], s[74:75], s[76:77]\n\t"
      "s_branch Lnext%=\n\t"
      "Lnxs%=:\n\t"
      "s_sub_u32 s68, s58, s89\n\t"
      "s_sub_u32 s69, s68, 1\n\t"
      "s_min_i32 s90, s69, 4\n\t"
      "s_sub_u32 s68, s68, s90\n\t"
      "s_cmp_ge_i32 s69, 0\n\t"
      "s_cselect_b32 s93, 0, s89\n\t"
      "s_cselect_b32 s89, s89, 0\n\t"
      "s_cmp_lg_u32 s89, 0\n\t"
      "s_cselect_b32 s60, s68, s58\n\t"
      "Lhs%=:\n\t"
      "s_add_u32 m0, s52, s54\n\t"
      "s_mov_b32 s52, s53\n\t"
      "s_add_u32 s53, s53, 16384\n\t"
      "s_cmp_eq_u32 s53, 98304\n\t"
      "s_cselect_b32 s53, 0, s53\n\t"
      "v_add_u32 v41, s52, %[vb0]\n\t"
      "v_add_u32 v42, s52, %[vb1]\n\t"
      "v_add_u32 v40, s53, %[kb]\n\t"
      "s_waitcnt vmcnt(12)\n\t"
      "s_barrier\n\t"
      "s_waitcnt lgkmcnt(5)\n\t"
      "v_mfma_f32_16x16x32_bf16 v[80:83], a[64:67], a[32:35], v[32:35]\n\t"
      "ds_read_b64_tr_b16 a[96:97], v41 offset:0\n\t"
      "ds_read_b64_tr_b16 a[98:99], v41 offset:8192\n\t"
      "v_mfma_f32_16x16x32_bf16 v[80:83], a[68:71], a[36:39], v[80:83]\n\t"
      "ds_read_b64_tr_b16 a[100:101], v42 offset:0\n\t"
      "ds_read_b64_tr_b16 a[102:103], v42 offset:8192\n\t"
      "v_mfma_f32_16x16x32_bf16 v[80:83], a[72:75], a[40:43], v[80:83]\n\t"
      "ds_read_b64_tr_b16 a[104:105], v41 offset:512\n\t"
      "ds_read_b64_tr_b16 a[106:107], v41 offset:8704\n\t"
      "s_waitcnt lgkmcnt(6)\n\t"
      "v_mfma_f32_16x16x32_bf16 v[80:83], a[76:79], a[44:47], v[80:83]\n\t"
      "ds_read_b64_tr_b16 a[108:109], v42 offset:512\n\t"
      "ds_read_b64_tr_b16 a[110:111], v42 offset:8704\n\t"
      "v_mfma_f32_16x16x32_bf16 v[88:91], a[64:67], a[48:51], 0\n\t"
      "ds_read_b128 a[64:67], v40 offset:0\n\t"
      "buffer_load_dwordx4 %[vk0], s[36:39], s55 offen lds\n\t"
      "v_mfma_f32_16x16x32_bf16 v[88:91], a[68:71], a[52:55], v[88:91]\n\t"
      "ds_read_b128 a[68:71], v40 offset:256\n\t"
      "buffer_load_dwordx4 %[vk1], s[36:39], s55 offen offset:1024 lds\n\t"
      "v_mfma_f32_16x16x32_bf16 v[88:91], a[72:75], a[56:59], v[88:91]\n\t"
      "ds_read_b128 a[72:75], v40 offset:512\n\t"
      "buffer_load_dwordx4 %[vv0], s[40:43], s55 offen offset:2048 lds\n\t"
      "v_mfma_f32_16x16x32_bf16 v[88:91], a[76:79], a[60:63], v[88:91]\n\t"
      "ds_read_b128 a[76:79], v40 offset:768\n\t"
      "buffer_load_dwordx4 %[vv1], s[40:43], s55 offen offset:3072 lds\n\t"
      "s_add_u32 s55, s55, s86\n\t"
      "v_mfma_f32_16x16x32_bf16 v[92:95], a[80:83], a[48:51], v[32:35]\n\t"
      "ds_read_b128 a[80:83], v40 offset:8192\n\t"
      "s_sub_u32 s56, s56, 1\n\t"
      "v_max3_f32 v44, v80, v81, v82\n\t"
      "v_mfma_f32_16x16x32_bf16 v[92:95], a[84:87], a[52:55], v[92:95]\n\t"
      "ds_read_b128 a[84:87], v40 offset:8448\n\t"
      "s_cbranch_scc1 Lps11_%=\n\t"
      "Lpr11_%=:\n\t"
      "v_max_f32 v44, v44, v83\n\t"
      "v_mfma_f32_16x16x32_bf16 v[92:95], a[88:91], a[56:59], v[92:95]\n\t"
      "ds_read_b128 a[88:91], v40 offset:8704\n\t"
      "s_cmp_lg_u32 s93, 0\n\t"
      "v_mov_b32 v45, v44\n\t"
      "v_mfma_f32_16x16x32_bf16 v[92:95], a[92:95], a[60:63], v[92:95]\n\t"
      "ds_read_b128 a[92:95], v40 offset:8960\n\t"
      "s_cbranch_scc1 Lqs12_%=\n\t"
      "Lqr12_%=:\n\t"
      "v_permlane16_swap_b32 v44, v45\n\t"
      "v_max_f32 v44, v44, v45\n\t"
      "v_mov_b32 v45, v44\n\t"
      "v_max3_f32 v46, v88, v89, v90\n\t"
      "s_add_u32 s70, s61, 0x10000\n\t"
      "v_permlane32_swap_b32 v44, v45\n\t"
      "v_max_f32 v44, v44, v45\n\t"
      "v_mul_f32 v38, 0xbe0293ee, v44\n\t"
      "v_max3_f32 v46, v46, v91, v92\n\t"
      "v_max3_f32 v46, v46, v93, v94\n\t"
      "v_max_f32 v46, v46, v95\n\t"
      "v_mov_b32 v47, v46\n\t"
      "buffer_store_dwordx4 v[132:135], v53, s[48:51], s61 offen sc1 nt\n\t"
      "s_nop 0\n\t"
      "v_permlane16_swap_b32 v46, v47\n\t"
      "v_max_f32 v46, v46, v47\n\t"
      "v_mov_b32 v47, v46\n\t"
      "v_fma_f32 v80, v80, s62, v38\n\t"
      "v_fma_f32 v81, v81, s62, v38\n\t"
      "v_permlane32_swap_b32 v46, v47\n\t"
      "v_max_f32 v46, v46, v47\n\t"
      "v_mul_f32 v39, 0xbe0293ee, v46\n\t"
      "v_fma_f32 v88, v88, s62, v39\n\t"
      "v_fma_f32 v89, v89, s62, v39\n\t"
      "v_exp_f32 v88, v88\n\t"
      "v_exp_f32 v89, v89\n\t"
      "v_fma_f32 v90, v90, s62, v39\n\t"
      "v_fma_f32 v91, v91, s62, v39\n\t"
      "v_exp_f32 v90, v90\n\t"
      "buffer_store_dwordx4 v[140:143], v54, s[48:51], s61 offen sc1 nt\n\t"
      "v_exp_f32 v91, v91\n\t"
      "v_exp_f32 v80, v80\n\t"
      "v_exp_f32 v81, v81\n\t"
      "v_fma_f32 v82, v82, s62, v38\n\t"
      "v_cvt_pk_bf16_f32 v104, v80, v81\n\t"
      "v_fma_f32 v83, v83, s62, v38\n\t"
      "v_exp_f32 v82, v82\n\t"
      "v_exp_f32 v83, v83\n\t"
      "v_mov_b32 v106, 0\n\t"
      "v_cvt_pk_bf16_f32 v105, v82, v83\n\t"
      "v_mov_b32 v107, 0\n\t"
      "buffer_store_dwordx4 v[148:151], v55, s[48:51], s61 offen sc1 nt\n\t"
      "buffer_store_dwordx4 v[156:159], v56, s[48:51], s61 offen sc1 nt\n\t"
      "s_mov_b32 s61, s78\n\t"
      "buffer_store_dwordx4 v[164:167], v53, s[48:51], s70 offen sc1 nt\n\t"
      "buffer_store_dwordx4 v[172:175], v54, s[48:51], s70 offen sc1 nt\n\t"
      "buffer_store_dwordx4 v[180:183], v55, s[48:51], s70 offen sc1 nt\n\t"
      "buffer_store_dwordx4 v[188:191], v56, s[48:51], s70 offen sc1 nt\n\t"
      "s_mov_b32 s50, s82\n\t"
      "s_sub_u32 s60, s60, 1\n\t"
      "s_cbranch_scc1 Lts1%=\n\t"
      "s_waitcnt lgkmcnt(1)\n\t"
      "v_mfma_f32_16x16x32_bf16 v[80:83], a[64:67], a[32:35], 0\n\t"
      "s_add_u32 m0, s52, s54\n\t"
      "v_cvt_pk_bf16_f32 v108, v88, v89\n\t"
      "v_cvt_pk_bf16_f32 v109, v90, v91\n\t"
      "v_mfma_f32_16x16x32_bf16 v[80:83], a[68:71], a[36:39], v[80:83]\n\t"
      "s_mov_b32 s52, s53\n\t"
      "ds_read_b64_tr_b16 a[112:113], v41 offset:1024\n\t"
      "v_mfma_f32_16x16x32_bf16 v[80:83], a[72:75], a[40:43], v[80:83]\n\t"
      "s_add_u32 s53, s53, 16384\n\t"
      "ds_read_b64_tr_b16 a[114:115], v41 offset:9216\n\t"
      "v_mfma_f32_16x16x32_bf16 v[80:83], a[76:79], a[44:47], v[80:83]\n\t"
      "s_cmp_eq_u32 s53, 98304\n\t"
      "ds_read_b64_tr_b16 a[116:117], v42 offset:1024\n\t"
      "v_mfma_f32_16x16x32_bf16 v[84:87], a[80:83], a[32:35], 0\n\t"
      "s_cselect_b32 s53, 0, s53\n\t"
      "v_add_u32 v40, s53, %[kb]\n\t"
      "v_fma_f32 v92, v92, s62, v39\n\t"
      "v_mfma_f32_16x16x32_bf16 v[84:87], a[84:87], a[36:39], v[84:87]\n\t"
      "ds_read_b64_tr_b16 a[118:119], v42 offset:9216\n\t"
      "v_fma_f32 v93, v93, s62, v39\n\t"
      "v_mfma_f32_16x16x32_bf16 v[84:87], a[88:91], a[40:43], v[84:87]\n\t"
      "ds_read_b64_tr_b16 a[120:121], v41 offset:1536\n\t"
      "v_fma_f32 v94, v94, s62, v39\n\t"
      "s_waitcnt lgkmcnt(5)\n\t"
      "v_mfma_f32_16x16x32_bf16 v[84:87], a[92:95], a[44:47], v[84:87]\n\t"
      "ds_read_b64_tr_b16 a[122:123], v41 offset:9728\n\t"
      "v_add_u32 v41, s52, %[vb0]\n\t"
      "v_mfma_f32_16x16x32_bf16 v[88:91], a[64:67], a[48:51], 0\n\t"
      "ds_read_b64_tr_b16 a[124:125], v42 offset:1536\n\t"
      "v_fma_f32 v95, v95, s62, v39\n\t"
      "v_mfma_f32_16x16x32_bf16 v[88:91], a[68:71], a[52:55], v[88:91]\n\t"
      "ds_read_b64_tr_b16 a[126:127], v42 offset:9728\n\t"
      "v_add_u32 v42, s52, %[vb1]\n\t"
      "v_mfma_f32_16x16x32_bf16 v[88:91], a[72:75], a[56:59], v[88:91]\n\t"
      "v_exp_f32 v92, v92\n\t"
      "v_mfma_f32_16x16x32_bf16 v[88:91], a[76:79], a[60:63], v[88:91]\n\t"
      "s_waitcnt vmcnt(20)\n\t"
      "v_exp_f32 v93, v93\n\t"
      "v_exp_f32 v94, v94\n\t"
      "v_cvt_pk_bf16_f32 v110, v92, v93\n\t"
      "v_exp_f32 v95, v95\n\t"
      "v_fma_f32 v80, v80, s62, v38\n\t"
      "v_cvt_pk_bf16_f32 v111, v94, v95\n\t"
      "v_mfma_f32_16x16x32_bf16 v[92:95], a[80:83], a[48:51], 0\n\t"
      "v_fma_f32 v81, v81, s62, v38\n\t"
      "v_fma_f32 v82, v82, s62, v38\n\t"
      "v_mfma_f32_16x16x32_bf16 v[92:95], a[84:87], a[52:55], v[92:95]\n\t"
      "v_exp_f32 v80, v80\n\t"
      "v_mfma_f32_16x16x32_bf16 v[92:95], a[88:91], a[56:59], v[92:95]\n\t"
      "v_exp_f32 v81, v81\n\t"
      "v_mfma_f32_16x16x32_bf16 v[92:95], a[92:95], a[60:63], v[92:95]\n\t"
      "s_waitcnt lgkmcnt(0)\n\t"
      "s_barrier\n\t"
      "buffer_load_dwordx4 %[vk0], s[36:39], s55 offen lds\n\t"
      "v_mfma_f32_16x16x32_bf16 v[192:195], a[96:99], v[104:107], 0\n\t"
      "ds_read_b128 a[64:67], v40 offset:0\n\t"
      "v_fma_f32 v83, v83, s62, v38\n\t"
      "v_mfma_f32_16x16x32_bf16 v[196:199], a[100:103], v[104:107], 0\n\t"
      "ds_read_b128 a[68:71], v40 offset:256\n\t"
      "v_fma_f32 v84, v84, s62, v38\n\t"
      "v_mfma_f32_16x16x32_bf16 v[200:203], a[104:107], v[104:107], 0\n\t"
      "ds_read_b128 a[72:75], v40 offset:512\n\t"
      "buffer_load_dwordx4 %[vk1], s[36:39], s55 offen offset:1024 lds\n\t"
      "v_mfma_f32_16x16x32_bf16 v[204:207], a[108:111], v[104:107], 0\n\t"
      "ds_read_b128 a[76:79], v40 offset:768\n\t"
      "v_fma_f32 v85, v85, s62, v38\n\t"
      "v_mfma_f32_16x16x32_bf16 v[208:211], a[112:115], v[104:107], 0\n\t"
      "ds_read_b128 a[80:83], v40 offset:8192\n\t"
      "v_fma_f32 v86, v86, s62, v38\n\t"
      "v_mfma_f32_16x16x32_bf16 v[212:215], a[116:119], v[104:107], 0\n\t"
      "ds_read_b128 a[84:87], v40 offset:8448\n\t"
      "buffer_load_dwordx4 %[vv0], s[40:43], s55 offen offset:2048 lds\n\t"
      "v_mfma_f32_16x16x32_bf16 v[216:219], a[120:123], v[104:107], 0\n\t"
      "ds_read_b128 a[88:91], v40 offset:8704\n\t"
      "v_fma_f32 v87, v87, s62, v38\n\t"
      "v_mfma_f32_16x16x32_bf16 v[220:223], a[124:127], v[104:107], 0\n\t"
      "ds_read_b128 a[92:95], v40 offset:8960\n\t"
      "v_fma_f32 v88, v88, s62, v39\n\t"
      "v_mfma_f32_16x16x32_bf16 a[140:143], a[128:131], v[104:107], 0\n\t"
      "buffer_load_dwordx4 %[vv1], s[40:43], s55 offen offset:3072 lds\n\t"
      "v_cvt_pk_bf16_f32 v104, v80, v81\n\t"
      "v_mfma_f32_16x16x32_bf16 v[224:227], a[96:99], v[108:111], 0\n\t"
      "s_add_u32 s55, s55, s86\n\t"
      "ds_read_b64_tr_b16 a[96:97], v41 offset:0\n\t"
      "v_mfma_f32_16x16x32_bf16 v[228:231], a[100:103], v[108:111], 0\n\t"
      "s_sub_u32 s56, s56, 1\n\t"
      "ds_read_b64_tr_b16 a[98:99], v41 offset:8192\n\t"
      "v_mfma_f32_16x16x32_bf16 v[232:235], a[104:107], v[108:111], 0\n\t"
      "s_cbranch_scc1 Lps14_%=\n\t"
      "Lpr14_%=:\n\t"
      "ds_read_b64_tr_b16 a[100:101], v42 offset:0\n\t"
      "v_mfma_f32_16x16x32_bf16 v[236:239], a[108:111], v[108:111], 0\n\t"
      "ds_read_b64_tr_b16 a[102:103], v42 offset:8192\n\t"
      "v_fma_f32 v89, v89, s62, v39\n\t"
      "v_mfma_f32_16x16x32_bf16 v[240:243], a[112:115], v[108:111], 0\n\t"
      "ds_read_b64_tr_b16 a[104:105], v41 offset:512\n\t"
      "v_fma_f32 v90, v90, s62, v39\n\t"
      "v_mfma_f32_16x16x32_bf16 v[244:247], a[116:119], v[108:111], 0\n\t"
      "ds_read_b64_tr_b16 a[106:107], v41 offset:8704\n\t"
      "v_fma_f32 v91, v91, s62, v39\n\t"
      "v_mfma_f32_16x16x32_bf16 v[248:251], a[120:123], v[108:111], 0\n\t"
      "ds_read_b64_tr_b16 a[108:109], v42 offset:512\n\t"
      "v_exp_f32 v82, v82\n\t"
      "v_mfma_f32_16x16x32_bf16 v[252:255], a[124:127], v[108:111], 0\n\t"
      "ds_read_b64_tr_b16 a[110:111], v42 offset:8704\n\t"
      "s_cmp_eq_u32 s60, 0\n\t"
      "v_exp_f32 v83, v83\n\t"
      "v_exp_f32 v84, v84\n\t"
      "v_cvt_pk_bf16_f32 v105, v82, v83\n\t"
      "v_exp_f32 v85, v85\n\t"
      "v_exp_f32 v86, v86\n\t"
      "v_cvt_pk_bf16_f32 v106, v84, v85\n\t"
      "v_exp_f32 v87, v87\n\t"
      "v_exp_f32 v88, v88\n\t"
      "v_cvt_pk_bf16_f32 v107, v86, v87\n\t"
      "v_exp_f32 v89, v89\n\t"
      "v_exp_f32 v90, v90\n\t"
      "v_exp_f32 v91, v91\n\t"
      "v_mfma_f32_16x16x32_bf16 a[144:147], a[128:131], v[108:111], 0\n\t"
      "s_cbranch_scc1 Lps%=\n\t"
      "Les%=:\n\t"
      "s_sub_u32 s60, s60, 1\n\t"
      "s_min_u32 s58, s60, s56\n\t"
      "s_sub_u32 s60, s60, s58\n\t"
      "s_sub_u32 s56, s56, s58\n\t"
      "Lls%=:\n\t"
      "s_waitcnt lgkmcnt(6)\n\t"
      "v_mfma_f32_16x16x32_bf16 v[80:83], a[64:67], a[32:35], 0\n\t"
      "s_add_u32 m0, s52, s54\n\t"
      "v_cvt_pk_bf16_f32 v108, v88, v89\n\t"
      "v_cvt_pk_bf16_f32 v109, v90, v91\n\t"
      "v_mfma_f32_16x16x32_bf16 v[80:83], a[68:71], a[36:39], v[80:83]\n\t"
      "s_mov_b32 s52, s53\n\t"
      "ds_read_b64_tr_b16 a[112:113], v41 offset:1024\n\t"
      "v_mfma_f32_16x16x32_bf16 v[80:83], a[72:75], a[40:43], v[80:83]\n\t"
      "s_add_u32 s53, s53, 16384\n\t"
      "ds_read_b64_tr_b16 a[114:115], v41 offset:9216\n\t"
      "v_mfma_f32_16x16x32_bf16 v[80:83], a[76:79], a[44:47], v[80:83]\n\t"
      "s_cmp_eq_u32 s53, 98304\n\t"
      "ds_read_b64_tr_b16 a[116:117], v42 offset:1024\n\t"
      "v_mfma_f32_16x16x32_bf16 v[84:87], a[80:83], a[32:35], 0\n\t"
      "s_cselect_b32 s53, 0, s53\n\t"
      "v_add_u32 v40, s53, %[kb]\n\t"
      "v_fma_f32 v92, v92, s62, v39\n\t"
      "v_mfma_f32_16x16x32_bf16 v[84:87], a[84:87], a[36:39], v[84:87]\n\t"
      "ds_read_b64_tr_b16 a[118:119], v42 offset:9216\n\t"
      "v_fma_f32 v93, v93, s62, v39\n\t"
      "v_mfma_f32_16x16x32_bf16 v[84:87], a[88:91], a[40:43], v[84:87]\n\t"
      "ds_read_b64_tr_b16 a[120:121], v41 offset:1536\n\t"
      "v_fma_f32 v94, v94, s62, v39\n\t"
      "v_mfma_f32_16x16x32_bf16 v[84:87], a[92:95], a[44:47], v[84:87]\n\t"
      "ds_read_b64_tr_b16 a[122:123], v41 offset:9728\n\t"
      "v_add_u32 v41, s52, %[vb0]\n\t"
      "v_mfma_f32_16x16x32_bf16 v[88:91], a[64:67], a[48:51], 0\n\t"
      "ds_read_b64_tr_b16 a[124:125], v42 offset:1536\n\t"
      "v_fma_f32 v95, v95, s62, v39\n\t"
      "v_mfma_f32_16x16x32_bf16 v[88:91], a[68:71], a[52:55], v[88:91]\n\t"
      "ds_read_b64_tr_b16 a[126:127], v42 offset:9728\n\t"
      "v_add_u32 v42, s52, %[vb1]\n\t"
      "v_mfma_f32_16x16x32_bf16 v[88:91], a[72:75], a[56:59], v[88:91]\n\t"
      "v_exp_f32 v92, v92\n\t"
      "v_mfma_f32_16x16x32_bf16 v[88:91], a[76:79], a[60:63], v[88:91]\n\t"
      "s_waitcnt vmcnt(12)\n\t"
      "v_exp_f32 v93, v93\n\t"
      "v_exp_f32 v94, v94\n\t"
      "v_cvt_pk_bf16_f32 v110, v92, v93\n\t"
      "v_exp_f32 v95, v95\n\t"
      "v_fma_f32 v80, v80, s62, v38\n\t"
      "v_cvt_pk_bf16_f32 v111, v94, v95\n\t"
      "v_mfma_f32_16x16x32_bf16 v[92:95], a[80:83], a[48:51], 0\n\t"
      "v_fma_f32 v81, v81, s62, v38\n\t"
      "v_fma_f32 v82, v82, s62, v38\n\t"
      "v_mfma_f32_16x16x32_bf16 v[92:95], a[84:87], a[52:55], v[92:95]\n\t"
      "v_exp_f32 v80, v80\n\t"
      "v_mfma_f32_16x16x32_bf16 v[92:95], a[88:91], a[56:59], v[92:95]\n\t"
      "v_exp_f32 v81, v81\n\t"
      "v_mfma_f32_16x16x32_bf16 v[92:95], a[92:95], a[60:63], v[92:95]\n\t"
      "s_waitcnt lgkmcnt(0)\n\t"
      "s_barrier\n\t"
      "buffer_load_dwordx4 %[vk0], s[36:39], s55 offen lds\n\t"
      "v_mfma_f32_16x16x32_bf16 v[192:195], a[96:99], v[104:107], v[192:195]\n\t"
      "ds_read_b128 a[64:67], v40 offset:0\n\t"
      "v_fma_f32 v83, v83, s62, v38\n\t"
      "v_mfma_f32_16x16x32_bf16 v[196:199], a[100:103], v[104:107], v[196:199]\n\t"
      "ds_read_b128 a[68:71], v40 offset:256\n\t"
      "v_fma_f32 v84, v84, s62, v38\n\t"
      "v_mfma_f32_16x16x32_bf16 v[200:203], a[104:107], v[104:107], v[200:203]\n\t"
      "ds_read_b128 a[72:75], v40 offset:512\n\t"
      "buffer_load_dwordx4 %[vk1], s[36:39], s55 offen offset:1024 lds\n\t"
      "v_mfma_f32_16x16x32_bf16 v[204:207], a[108:111], v[104:107], v[204:207]\n\t"
      "ds_read_b128 a[76:79], v40 offset:768\n\t"
      "v_fma_f32 v85, v85, s62, v38\n\t"
      "v_mfma_f32_16x16x32_bf16 v[208:211], a[112:115], v[104:107], v[208:211]\n\t"
      "ds_read_b128 a[80:83], v40 offset:8192\n\t"
      "v_fma_f32 v86, v86, s62, v38\n\t"
      "v_mfma_f32_16x16x32_bf16 v[212:215], a[116:119], v[104:107], v[212:215]\n\t"
      "ds_read_b128 a[84:87], v40 offset:8448\n\t"
      "buffer_load_dwordx4 %[vv0], s[40:43], s55 offen offset:2048 lds\n\t"
      "v_mfma_f32_16x16x32_bf16 v[216:219], a[120:123], v[104:107], v[216:219]\n\t"
      "ds_read_b128 a[88:91], v40 offset:8704\n\t"
      "v_fma_f32 v87, v87, s62, v38\n\t"
      "v_mfma_f32_16x16x32_bf16 v[220:223], a[124:127], v[104:107], v[220:223]\n\t"
      "ds_read_b128 a[92:95], v40 offset:8960\n\t"
      "v_fma_f32 v88, v88, s62, v39\n\t"
      "v_mfma_f32_16x16x32_bf16 a[140:143], a[128:131], v[104:107], a[140:143]\n\t"
      "buffer_load_dwordx4 %[vv1], s[40:43], s55 offen offset:3072 lds\n\t"
      "v_cvt_pk_bf16_f32 v104, v80, v81\n\t"
      "v_mfma_f32_16x16x32_bf16 v[224:227], a[96:99], v[108:111], v[224:227]\n\t"
      "s_add_u32 s55, s55, s86\n\t"
      "ds_read_b64_tr_b16 a[96:97], v41 offset:0\n\t"
      "v_mfma_f32_16x16x32_bf16 v[228:231], a[100:103], v[108:111], v[228:231]\n\t"
      "ds_read_b64_tr_b16 a[98:99], v41 offset:8192\n\t"
      "v_fma_f32 v89, v89, s62, v39\n\t"
      "v_mfma_f32_16x16x32_bf16 v[232:235], a[104:107], v[108:111], v[232:235]\n\t"
      "ds_read_b64_tr_b16 a[100:101], v42 offset:0\n\t"
      "v_fma_f32 v90, v90, s62, v39\n\t"
      "v_mfma_f32_16x16x32_bf16 v[236:239], a[108:111], v[108:111], v[236:239]\n\t"
      "ds_read_b64_tr_b16 a[102:103], v42 offset:8192\n\t"
      "v_fma_f32 v91, v91, s62, v39\n\t"
      "v_mfma_f32_16x16x32_bf16 v[240:243], a[112:115], v[108:111], v[240:243]\n\t"
      "ds_read_b64_tr_b16 a[104:105], v41 offset:512\n\t"
      "v_exp_f32 v82, v82\n\t"
      "v_mfma_f32_16x16x32_bf16 v[244:247], a[116:119], v[108:111], v[244:247]\n\t"
      "ds_read_b64_tr_b16 a[106:107], v41 offset:8704\n\t"
      "v_exp_f32 v83, v83\n\t"
      "v_mfma_f32_16x16x32_bf16 v[248:251], a[120:123], v[108:111], v[248:251]\n\t"
      "ds_read_b64_tr_b16 a[108:109], v42 offset:512\n\t"
      "v_cvt_pk_bf16_f32 v105, v82, v83\n\t"
      "v_mfma_f32_16x16x32_bf16 v[252:255], a[124:127], v[108:111], v[252:255]\n\t"
      "ds_read_b64_tr_b16 a[110:111], v42 offset:8704\n\t"
      "s_sub_u32 s58, s58, 1\n\t"
      "v_exp_f32 v84, v84\n\t"
      "v_exp_f32 v85, v85\n\t"
      "v_exp_f32 v86, v86\n\t"
      "v_cvt_pk_bf16_f32 v106, v84, v85\n\t"
      "v_exp_f32 v87, v87\n\t"
      "v_exp_f32 v88, v88\n\t"
      "v_cvt_pk_bf16_f32 v107, v86, v87\n\t"
      "v_exp_f32 v89, v89\n\t"
      "v_exp_f32 v90, v90\n\t"
      "v_exp_f32 v91, v91\n\t"
      "v_mfma_f32_16x16x32_bf16 a[144:147], a[128:131], v[108:111], a[144:147]\n\t"
      "s_cbranch_scc0 Lls%=\n\t"
      "s_sub_u32 s56, s56, 1\n\t"
      "s_cbranch_scc1 Lps16_%=\n\t"
      "Lpr16_%=:\n\t"
      "s_cmp_lg_u32 s60, 0\n\t"
      "s_cbranch_scc1 Les%=\n\t"
      "Lps%=:\n\t"
      "s_sub_u32 s58, s89, 1\n\t"
      "s_cbranch_scc1 Lts%=\n\t"
      "s_mov_b32 s89, 0\n\t"
      "Lqs%=:\n\t"
      "s_waitcnt lgkmcnt(7)\n\t"
      "v_mfma_f32_16x16x32_bf16 v[80:83], a[64:67], a[32:35], 0\n\t"
      "s_add_u32 m0, s91, 0\n\t"
      "v_cvt_pk_bf16_f32 v108, v88, v89\n\t"
      "v_cvt_pk_bf16_f32 v109, v90, v91\n\t"
      "v_mfma_f32_16x16x32_bf16 v[80:83], a[68:71], a[36:39], v[80:83]\n\t"
      "buffer_load_dwordx4 %[vqa], s[44:47], s92 offen offset:0  lds\n\t"
      "ds_read_b64_tr_b16 a[112:113], v41 offset:1024\n\t"
      "v_mfma_f32_16x16x32_bf16 v[80:83], a[72:75], a[40:43], v[80:83]\n\t"
      "s_add_u32 s92, s92, 15360\n\t"
      "buffer_load_dwordx4 %[vqa], s[44:47], s92 offen offset:1024  lds\n\t"
      "v_mfma_f32_16x16x32_bf16 v[80:83], a[76:79], a[44:47], v[80:83]\n\t"
      "s_add_u32 s92, s92, 15360\n\t"
      "buffer_load_dwordx4 %[vqb], s[44:47], s92 offen offset:2048  lds\n\t"
      "v_mfma_f32_16x16x32_bf16 v[84:87], a[80:83], a[32:35], 0\n\t"
      "s_add_u32 s92, s92, 15360\n\t"
      "buffer_load_dwordx4 %[vqb], s[44:47], s92 offen offset:3072  lds\n\t"
      "v_mfma_f32_16x16x32_bf16 v[84:87], a[84:87], a[36:39], v[84:87]\n\t"
      "s_add_u32 s92, s92, 19456\n\t"
      "ds_read_b64_tr_b16 a[114:115], v41 offset:9216\n\t"
      "v_mfma_f32_16x16x32_bf16 v[84:87], a[88:91], a[40:43], v[84:87]\n\t"
      "s_add_u32 m0, s91, 4096\n\t"
      "ds_read_b64_tr_b16 a[116:117], v42 offset:1024\n\t"
      "v_mfma_f32_16x16x32_bf16 v[84:87], a[92:95], a[44:47], v[84:87]\n\t"
      "buffer_load_dwordx4 %[vqa], s[44:47], s92 offen offset:0  lds\n\t"
      "ds_read_b64_tr_b16 a[118:119], v42 offset:9216\n\t"
      "v_mfma_f32_16x16x32_bf16 v[88:91], a[64:67], a[48:51], 0\n\t"
      "s_add_u32 s92, s92, 15360\n\t"
      "buffer_load_dwordx4 %[vqa], s[44:47], s92 offen offset:1024  lds\n\t"
      "v_mfma_f32_16x16x32_bf16 v[88:91], a[68:71], a[52:55], v[88:91]\n\t"
      "s_add_u32 s92, s92, 15360\n\t"
      "ds_read_b64_tr_b16 a[120:121], v41 offset:1536\n\t"
      "v_mfma_f32_16x16x32_bf16 v[88:91], a[72:75], a[56:59], v[88:91]\n\t"
      "buffer_load_dwordx4 %[vqb], s[44:47], s92 offen offset:2048  lds\n\t"
      "ds_read_b64_tr_b16 a[122:123], v41 offset:9728\n\t"
      "v_mfma_f32_16x16x32_bf16 v[88:91], a[76:79], a[60:63], v[88:91]\n\t"
      "s_add_u32 s92, s92, 15360\n\t"
      "buffer_load_dwordx4 %[vqb], s[44:47], s92 offen offset:3072  lds\n\t"
      "s_sub_u32 s92, s92, 242688\n\t"
      "s_sub_u32 s91, s91, 8192\n\t"
      "s_add_u32 m0, s52, s54\n\t"
      "s_mov_b32 s52, s53\n\t"
      "s_add_u32 s53, s53, 16384\n\t"
      "s_cmp_eq_u32 s53, 98304\n\t"
      "s_cselect_b32 s53, 0, s53\n\t"
      "v_add_u32 v41, s52, %[vb0]\n\t"
      "v_add_u32 v40, s53, %[kb]\n\t"
      "v_fma_f32 v92, v92, s62, v39\n\t"
      "v_fma_f32 v93, v93, s62, v39\n\t"
      "v_exp_f32 v92, v92\n\t"
      "v_exp_f32 v93, v93\n\t"
      "v_fma_f32 v94, v94, s62, v39\n\t"
      "v_cvt_pk_bf16_f32 v110, v92, v93\n\t"
      "v_fma_f32 v95, v95, s62, v39\n\t"
      "v_exp_f32 v94, v94\n\t"
      "v_exp_f32 v95, v95\n\t"
      "v_fma_f32 v80, v80, s62, v38\n\t"
      "v_cvt_pk_bf16_f32 v111, v94, v95\n\t"
      "v_mfma_f32_16x16x32_bf16 v[92:95], a[80:83], a[48:51], 0\n\t"
      "s_waitcnt vmcnt(20)\n\t"
      "ds_read_b64_tr_b16 a[124:125], v42 offset:1536\n\t"
      "v_mfma_f32_16x16x32_bf16 v[92:95], a[84:87], a[52:55], v[92:95]\n\t"
      "ds_read_b64_tr_b16 a[126:127], v42 offset:9728\n\t"
      "v_add_u32 v42, s52, %[vb1]\n\t"
      "v_mfma_f32_16x16x32_bf16 v[92:95], a[88:91], a[56:59], v[92:95]\n\t"
      "v_fma_f32 v81, v81, s62, v38\n\t"
      "v_fma_f32 v82, v82, s62, v38\n\t"
      "v_mfma_f32_16x16x32_bf16 v[92:95], a[92:95], a[60:63], v[92:95]\n\t"
      "s_waitcnt lgkmcnt(4)\n\t"
      "v_exp_f32 v80, v80\n\t"
      "v_mfma_f32_16x16x32_bf16 v[192:195], a[96:99], v[104:107], v[192:195]\n\t"
      "v_exp_f32 v81, v81\n\t"
      "v_mfma_f32_16x16x32_bf16 v[196:199], a[100:103], v[104:107], v[196:199]\n\t"
      "v_fma_f32 v83, v83, s62, v38\n\t"
      "v_fma_f32 v84, v84, s62, v38\n\t"
      "v_mfma_f32_16x16x32_bf16 v[200:203], a[104:107], v[104:107], v[200:203]\n\t"
      "v_exp_f32 v82, v82\n\t"
      "v_mfma_f32_16x16x32_bf16 v[204:207], a[108:111], v[104:107], v[204:207]\n\t"
      "v_exp_f32 v83, v83\n\t"
      "v_mfma_f32_16x16x32_bf16 v[208:211], a[112:115], v[104:107], v[208:211]\n\t"
      "v_fma_f32 v85, v85, s62, v38\n\t"
      "v_fma_f32 v86, v86, s62, v38\n\t"
      "v_mfma_f32_16x16x32_bf16 v[212:215], a[116:119], v[104:107], v[212:215]\n\t"
      "s_waitcnt lgkmcnt(0)\n\t"
      "s_barrier\n\t"
      "ds_read_b128 a[64:67], v40 offset:0\n\t"
      "v_mfma_f32_16x16x32_bf16 v[216:219], a[120:123], v[104:107], v[216:219]\n\t"
      "ds_read_b128 a[68:71], v40 offset:256\n\t"
      "buffer_load_dwordx4 %[vk0], s[36:39], s55 offen lds\n\t"
      "v_mfma_f32_16x16x32_bf16 v[220:223], a[124:127], v[104:107], v[220:223]\n\t"
      "ds_read_b128 a[72:75], v40 offset:512\n\t"
      "buffer_load_dwordx4 %[vk1], s[36:39], s55 offen offset:1024 lds\n\t"
      "v_mfma_f32_16x16x32_bf16 a[140:143], a[128:131], v[104:107], a[140:143]\n\t"
      "ds_read_b128 a[76:79], v40 offset:768\n\t"
      "buffer_load_dwordx4 %[vv0], s[40:43], s55 offen offset:2048 lds\n\t"
      "v_mfma_f32_16x16x32_bf16 v[224:227], a[96:99], v[108:111], v[224:227]\n\t"
      "ds_read_b128 a[80:83], v40 offset:8192\n\t"
      "buffer_load_dwordx4 %[vv1], s[40:43], s55 offen offset:3072 lds\n\t"
      "v_mfma_f32_16x16x32_bf16 v[228:231], a[100:103], v[108:111], v[228:231]\n\t"
      "ds_read_b128 a[84:87], v40 offset:8448\n\t"
      "v_cvt_pk_bf16_f32 v104, v80, v81\n\t"
      "v_mfma_f32_16x16x32_bf16 v[232:235], a[104:107], v[108:111], v[232:235]\n\t"
      "ds_read_b128 a[88:91], v40 offset:8704\n\t"
      "v_cvt_pk_bf16_f32 v105, v82, v83\n\t"
      "v_mfma_f32_16x16x32_bf16 v[236:239], a[108:111], v[108:111], v[236:239]\n\t"
      "ds_read_b128 a[92:95], v40 offset:8960\n\t"
      "v_fma_f32 v87, v87, s62, v38\n\t"
      "v_mfma_f32_16x16x32_bf16 v[240:243], a[112:115], v[108:111], v[240:243]\n\t"
      "s_add_u32 s55, s55, s86\n\t"
      "ds_read_b64_tr_b16 a[96:97], v41 offset:0\n\t"
      "v_mfma_f32_16x16x32_bf16 v[244:247], a[116:119], v[108:111], v[244:247]\n\t"
      "s_sub_u32 s56, s56, 1\n\t"
      "ds_read_b64_tr_b16 a[98:99], v41 offset:8192\n\t"
      "v_mfma_f32_16x16x32_bf16 v[248:251], a[120:123], v[108:111], v[248:251]\n\t"
      "s_cbranch_scc1 Lps18_%=\n\t"
      "Lpr18_%=:\n\t"
      "ds_read_b64_tr_b16 a[100:101], v42 offset:0\n\t"
      "v_mfma_f32_16x16x32_bf16 v[252:255], a[124:127], v[108:111], v[252:255]\n\t"
      "ds_read_b64_tr_b16 a[102:103], v42 offset:8192\n\t"
      "s_sub_u32 s58, s58, 1\n\t"
      "v_exp_f32 v84, v84\n\t"
      "v_exp_f32 v85, v85\n\t"
      "v_exp_f32 v86, v86\n\t"
      "v_cvt_pk_bf16_f32 v106, v84, v85\n\t"
      "v_exp_f32 v87, v87\n\t"
      "v_fma_f32 v88, v88, s62, v39\n\t"
      "v_cvt_pk_bf16_f32 v107, v86, v87\n\t"
      "v_fma_f32 v89, v89, s62, v39\n\t"
      "v_exp_f32 v88, v88\n\t"
      "v_exp_f32 v89, v89\n\t"
      "v_fma_f32 v90, v90, s62, v39\n\t"
      "v_fma_f32 v91, v91, s62, v39\n\t"
      "v_exp_f32 v90, v90\n\t"
      "v_exp_f32 v91, v91\n\t"
      "ds_read_b64_tr_b16 a[104:105], v41 offset:512\n\t"
      "ds_read_b64_tr_b16 a[106:107], v41 offset:8704\n\t"
      "ds_read_b64_tr_b16 a[108:109], v42 offset:512\n\t"
      "ds_read_b64_tr_b16 a[110:111], v42 offset:8704\n\t"
      "v_mfma_f32_16x16x32_bf16 a[144:147], a[128:131], v[108:111], a[144:147]\n\t"
      "s_cbranch_scc0 Lqs%=\n\t"
      "s_mov_b32 s60, s90\n\t"
      "s_cmp_lg_u32 s90, 0\n\t"
      "s_cbranch_scc1 Les%=\n\t"
      "Lts%=:\n\t"
      "s_waitcnt vmcnt(4)\n\t"
      "s_mov_b64 s[72:73], 0\n\t"
      "s_waitcnt lgkmcnt(6)\n\t"
      "v_mfma_f32_16x16x32_bf16 v[192:195], a[96:99], v[104:107], v[192:195]\n\t"
      "ds_read_b64_tr_b16 a[112:113], v41 offset:1024\n\t"
      "v_cvt_pk_bf16_f32 v108, v88, v89\n\t"
      "s_waitcnt lgkmcnt(5)\n\t"
      "v_mfma_f32_16x16x32_bf16 v[196:199], a[100:103], v[104:107], v[196:199]\n\t"
      "ds_read_b64_tr_b16 a[114:115], v41 offset:9216\n\t"
      "v_cvt_pk_bf16_f32 v109, v90, v91\n\t"
      "s_waitcnt lgkmcnt(4)\n\t"
      "v_mfma_f32_16x16x32_bf16 v[200:203], a[104:107], v[104:107], v[200:203]\n\t"
      "ds_read_b64_tr_b16 a[116:117], v42 offset:1024\n\t"
      "v_fma_f32 v92, v92, s62, v39\n\t"
      "s_waitcnt lgkmcnt(3)\n\t"
      "v_mfma_f32_16x16x32_bf16 v[204:207], a[108:111], v[104:107], v[204:207]\n\t"
      "ds_read_b64_tr_b16 a[118:119], v42 offset:9216\n\t"
      "v_fma_f32 v93, v93, s62, v39\n\t"
      "v_exp_f32 v92, v92\n\t"
      "v_exp_f32 v93, v93\n\t"
      "v_fma_f32 v94, v94, s62, v39\n\t"
      "v_cvt_pk_bf16_f32 v110, v92, v93\n\t"
      "v_fma_f32 v95, v95, s62, v39\n\t"
      "v_exp_f32 v94, v94\n\t"
      "v_exp_f32 v95, v95\n\t"
      "s_nop 0\n\t"
      "v_cvt_pk_bf16_f32 v111, v94, v95\n\t"
      "ds_read_b64_tr_b16 a[120:121], v41 offset:1536\n\t"
      "ds_read_b64_tr_b16 a[122:123], v41 offset:9728\n\t"
      "s_waitcnt lgkmcnt(4)\n\t"
      "v_mfma_f32_16x16x32_bf16 v[208:211], a[112:115], v[104:107], v[208:211]\n\t"
      "ds_read_b64_tr_b16 a[124:125], v42 offset:1536\n\t"
      "ds_read_b64_tr_b16 a[126:127], v42 offset:9728\n\t"
      "s_waitcnt lgkmcnt(4)\n\t"
      "v_mfma_f32_16x16x32_bf16 v[212:215], a[116:119], v[104:107], v[212:215]\n\t"
      "ds_read_b128 a[32:35], %[qb] offset:8192\n\t"
      "ds_read_b128 a[36:39], %[qb] offset:8448\n\t"
      "ds_read_b128 a[40:43], %[qb] offset:8704\n\t"
      "ds_read_b128 a[44:47], %[qb] offset:8960\n\t"
      "ds_read_b128 a[48:51], %[qb] offset:12288\n\t"
      "ds_read_b128 a[52:55], %[qb] offset:12544\n\t"
      "ds_read_b128 a[56:59], %[qb] offset:12800\n\t"
      "s_waitcnt lgkmcnt(9)\n\t"
      "v_mfma_f32_16x16x32_bf16 v[216:219], a[120:123], v[104:107], v[216:219]\n\t"
      "ds_read_b128 a[60:63], %[qb] offset:13056\n\t"
      "s_waitcnt lgkmcnt(8)\n\t"
      "v_mfma_f32_16x16x32_bf16 v[220:223], a[124:127], v[104:107], v[220:223]\n\t"
      "v_mfma_f32_16x16x32_bf16 a[140:143], a[128:131], v[104:107], a[140:143]\n\t"
      "v_mfma_f32_16x16x32_bf16 v[224:227], a[96:99], v[108:111], v[224:227]\n\t"
      "v_mfma_f32_16x16x32_bf16 v[228:231], a[100:103], v[108:111], v[228:231]\n\t"
      "s_nop 5\n\t"
      "v_accvgpr_read_b32 v48, a140\n\t"
      "v_cmp_ngt_f32 vcc, s63, v48\n\t"
      "v_mfma_f32_16x16x32_bf16 v[232:235], a[104:107], v[108:111], v[232:235]\n\t"
      "s_or_b64 s[72:73], s[72:73], vcc\n\t"
      "v_rcp_f32 v48, v48\n\t"
      "v_mfma_f32_16x16x32_bf16 v[236:239], a[108:111], v[108:111], v[236:239]\n\t"
      "v_mov_b32 v49, v48\n\t"
      "v_pk_mul_f32 v[192:193], v[192:193], v[48:49]\n\t"
      "v_mfma_f32_16x16x32_bf16 v[240:243], a[112:115], v[108:111], v[240:243]\n\t"
      "v_pk_mul_f32 v[194:195], v[194:195], v[48:49]\n\t"
      "v_cvt_pk_bf16_f32 v132, v192, v193\n\t"
      "v_mfma_f32_16x16x32_bf16 v[244:247], a[116:119], v[108:111], v[244:247]\n\t"
      "v_cvt_pk_bf16_f32 v133, v194, v195\n\t"
      "v_pk_mul_f32 v[196:197], v[196:197], v[48:49]\n\t"
      "v_mfma_f32_16x16x32_bf16 v[248:251], a[120:123], v[108:111], v[248:251]\n\t"
      "ds_write_b64 v112, v[132:133] offset:8192\n\t"
      "v_mfma_f32_16x16x32_bf16 v[252:255], a[124:127], v[108:111], v[252:255]\n\t"
      "v_pk_mul_f32 v[198:199], v[198:199], v[48:49]\n\t"
      "v_cvt_pk_bf16_f32 v134, v196, v197\n\t"
      "v_mfma_f32_16x16x32_bf16 a[144:147], a[128:131], v[108:111], a[144:147]\n\t"
      "v_cvt_pk_bf16_f32 v135, v198, v199\n\t"
      "ds_write_b64 v113, v[134:135] offset:8192\n\t"
      "v_pk_mul_f32 v[200:201], v[200:201], v[48:49]\n\t"
      "v_pk_mul_f32 v[202:203], v[202:203], v[48:49]\n\t"
      "v_cvt_pk_bf16_f32 v140, v200, v201\n\t"
      "v_cvt_pk_bf16_f32 v141, v202, v203\n\t"
      "ds_write_b64 v114, v[140:141] offset:8192\n\t"
      "v_pk_mul_f32 v[204:205], v[204:205], v[48:49]\n\t"
      "v_pk_mul_f32 v[206:207], v[206:207], v[48:49]\n\t"
      "v_cvt_pk_bf16_f32 v142, v204, v205\n\t"
      "v_cvt_pk_bf16_f32 v143, v206, v207\n\t"
      "ds_write_b64 v115, v[142:143] offset:8192\n\t"
      "v_pk_mul_f32 v[208:209], v[208:209], v[48:49]\n\t"
      "v_pk_mul_f32 v[210:211], v[210:211], v[48:49]\n\t"
      "v_cvt_pk_bf16_f32 v148, v208, v209\n\t"
      "v_cvt_pk_bf16_f32 v149, v210, v211\n\t"
      "ds_write_b64 v116, v[148:149] offset:8192\n\t"
      "v_pk_mul_f32 v[212:213], v[212:213], v[48:49]\n\t"
      "v_pk_mul_f32 v[214:215], v[214:215], v[48:49]\n\t"
      "v_cvt_pk_bf16_f32 v150, v212, v213\n\t"
      "v_cvt_pk_bf16_f32 v151, v214, v215\n\t"
      "ds_write_b64 v117, v[150:151] offset:8192\n\t"
      "v_pk_mul_f32 v[216:217], v[216:217], v[48:49]\n\t"
      "v_pk_mul_f32 v[218:219], v[218:219], v[48:49]\n\t"
      "v_cvt_pk_bf16_f32 v156, v216, v217\n\t"
      "v_cvt_pk_bf16_f32 v157, v218, v219\n\t"
      "ds_write_b64 v118, v[156:157] offset:8192\n\t"
      "v_pk_mul_f32 v[220:221], v[220:221], v[48:49]\n\t"
      "v_pk_mul_f32 v[222:223], v[222:223], v[48:49]\n\t"
      "v_cvt_pk_bf16_f32 v158, v220, v221\n\t"
      "v_cvt_pk_bf16_f32 v159, v222, v223\n\t"
      "ds_write_b64 v119, v[158:159] offset:8192\n\t"
      "ds_read_b128 v[132:135], v120 offset:8192\n\t"
      "v_accvgpr_read_b32 v50, a144\n\t"
      "ds_read_b128 v[140:143], v121 offset:8192\n\t"
      "v_cmp_ngt_f32 vcc, s63, v50\n\t"
      "ds_read_b128 v[148:151], v122 offset:8192\n\t"
      "s_or_b64 s[72:73], s[72:73], vcc\n\t"
      "v_rcp_f32 v50, v50\n\t"
      "ds_read_b128 v[156:159], v123 offset:8192\n\t"
      "v_mov_b32 v51, v50\n\t"
      "v_pk_mul_f32 v[224:225], v[224:225], v[50:51]\n\t"
      "v_pk_mul_f32 v[226:227], v[226:227], v[50:51]\n\t"
      "v_cvt_pk_bf16_f32 v164, v224, v225\n\t"
      "v_cvt_pk_bf16_f32 v165, v226, v227\n\t"
      "ds_write_b64 v112, v[164:165] offset:12288\n\t"
      "v_pk_mul_f32 v[228:229], v[228:229], v[50:51]\n\t"
      "v_pk_mul_f32 v[230:231], v[230:231], v[50:51]\n\t"
      "v_cvt_pk_bf16_f32 v166, v228, v229\n\t"
      "v_cvt_pk_bf16_f32 v167, v230, v231\n\t"
      "ds_write_b64 v113, v[166:167] offset:12288\n\t"
      "v_pk_mul_f32 v[232:233], v[232:233], v[50:51]\n\t"
      "v_pk_mul_f32 v[234:235], v[234:235], v[50:51]\n\t"
      "v_cvt_pk_bf16_f32 v172, v232, v233\n\t"
      "v_cvt_pk_bf16_f32 v173, v234, v235\n\t"
      "ds_write_b64 v114, v[172:173] offset:12288\n\t"
      "v_pk_mul_f32 v[236:237], v[236:237], v[50:51]\n\t"
      "v_pk_mul_f32 v[238:239], v[238:239], v[50:51]\n\t"
      "v_cvt_pk_bf16_f32 v174, v236, v237\n\t"
      "v_cvt_pk_bf16_f32 v175, v238, v239\n\t"
      "ds_write_b64 v115, v[174:175] offset:12288\n\t"
      "v_pk_mul_f32 v[240:241], v[240:241], v[50:51]\n\t"
      "v_pk_mul_f32 v[242:243], v[242:243], v[50:51]\n\t"
      "v_cvt_pk_bf16_f32 v180, v240, v241\n\t"
      "v_cvt_pk_bf16_f32 v181, v242, v243\n\t"
      "ds_write_b64 v116, v[180:181] offset:12288\n\t"
      "v_pk_mul_f32 v[244:245], v[244:245], v[50:51]\n\t"
      "v_pk_mul_f32 v[246:247], v[246:247], v[50:51]\n\t"
      "v_cvt_pk_bf16_f32 v182, v244, v245\n\t"
      "v_cvt_pk_bf16_f32 v183, v246, v247\n\t"
      "ds_write_b64 v117, v[182:183] offset:12288\n\t"
      "v_pk_mul_f32 v[248:249], v[248:249], v[50:51]\n\t"
      "v_pk_mul_f32 v[250:251], v[250:251], v[50:51]\n\t"
      "v_cvt_pk_bf16_f32 v188, v248, v249\n\t"
      "v_cvt_pk_bf16_f32 v189, v250, v251\n\t"
      "ds_write_b64 v118, v[188:189] offset:12288\n\t"
      "v_pk_mul_f32 v[252:253], v[252:253], v[50:51]\n\t"
      "v_pk_mul_f32 v[254:255], v[254:255], v[50:51]\n\t"
      "v_cvt_pk_bf16_f32 v190, v252, v253\n\t"
      "v_cvt_pk_bf16_f32 v191, v254, v255\n\t"
      "ds_write_b64 v119, v[190:191] offset:12288\n\t"
      "ds_read_b128 v[164:167], v120 offset:12288\n\t"
      "ds_read_b128 v[172:175], v121 offset:12288\n\t"
      "ds_read_b128 v[180:183], v122 offset:12288\n\t"
      "ds_read_b128 v[188:191], v123 offset:12288\n\t"
      "s_cmp_lg_u64 s[72:73], 0\n\t"
      "s_cselect_b32 s76, 1, 0\n\t"
      "s_mov_b32 s77, 0\n\t"
      "s_lshl_b64 s[76:77], s[76:77], s59\n\t"
      "s_or_b64 s[74:75], s[74:75], s[76:77]\n\t"
      "s_branch Lnext%=\n\t"
      "Lts1%=:\n\t"
      "s_waitcnt vmcnt(4)\n\t"
      "s_mov_b64 s[72:73], 0\n\t"
      "s_waitcnt lgkmcnt(1)\n\t"
      "v_mfma_f32_16x16x32_bf16 v[192:195], a[96:99], v[104:107], 0\n\t"
      "ds_read_b64_tr_b16 a[112:113], v41 offset:1024\n\t"
      "v_cvt_pk_bf16_f32 v108, v88, v89\n\t"
      "v_mfma_f32_16x16x32_bf16 v[196:199], a[100:103], v[104:107], 0\n\t"
      "ds_read_b64_tr_b16 a[114:115], v41 offset:9216\n\t"
      "v_cvt_pk_bf16_f32 v109, v90, v91\n\t"
      "v_mfma_f32_16x16x32_bf16 v[200:203], a[104:107], v[104:107], 0\n\t"
      "ds_read_b64_tr_b16 a[116:117], v42 offset:1024\n\t"
      "v_fma_f32 v92, v92, s62, v39\n\t"
      "v_mfma_f32_16x16x32_bf16 v[204:207], a[108:111], v[104:107], 0\n\t"
      "ds_read_b64_tr_b16 a[118:119], v42 offset:9216\n\t"
      "v_fma_f32 v93, v93, s62, v39\n\t"
      "v_exp_f32 v92, v92\n\t"
      "v_exp_f32 v93, v93\n\t"
      "v_fma_f32 v94, v94, s62, v39\n\t"
      "v_cvt_pk_bf16_f32 v110, v92, v93\n\t"
      "v_fma_f32 v95, v95, s62, v39\n\t"
      "v_exp_f32 v94, v94\n\t"
      "v_exp_f32 v95, v95\n\t"
      "s_nop 0\n\t"
      "v_cvt_pk_bf16_f32 v111, v94, v95\n\t"
      "ds_read_b64_tr_b16 a[120:121], v41 offset:1536\n\t"
      "ds_read_b64_tr_b16 a[122:123], v41 offset:9728\n\t"
      "s_waitcnt lgkmcnt(4)\n\t"
      "v_mfma_f32_16x16x32_bf16 v[208:211], a[112:115], v[104:107], 0\n\t"
      "ds_read_b64_tr_b16 a[124:125], v42 offset:1536\n\t"
      "ds_read_b64_tr_b16 a[126:127], v42 offset:9728\n\t"
      "s_waitcnt lgkmcnt(4)\n\t"
      "v_mfma_f32_16x16x32_bf16 v[212:215], a[116:119], v[104:107], 0\n\t"
      "ds_read_b128 a[32:35], %[qb] offset:8192\n\t"
      "ds_read_b128 a[36:39], %[qb] offset:8448\n\t"
      "ds_read_b128 a[40:43], %[qb] offset:8704\n\t"
      "ds_read_b128 a[44:47], %[qb] offset:8960\n\t"
      "ds_read_b128 a[48:51], %[qb] offset:12288\n\t"
      "ds_read_b128 a[52:55], %[qb] offset:12544\n\t"
      "ds_read_b128 a[56:59], %[qb] offset:12800\n\t"
      "s_waitcnt lgkmcnt(9)\n\t"
      "v_mfma_f32_16x16x32_bf16 v[216:219], a[120:123], v[104:107], 0\n\t"
      "ds_read_b128 a[60:63], %[qb] offset:13056\n\t"
      "s_waitcnt lgkmcnt(8)\n\t"
      "v_mfma_f32_16x16x32_bf16 v[220:223], a[124:127], v[104:107], 0\n\t"
      "v_mfma_f32_16x16x32_bf16 a[140:143], a[128:131], v[104:107], 0\n\t"
      "v_mfma_f32_16x16x32_bf16 v[224:227], a[96:99], v[108:111], 0\n\t"
      "v_mfma_f32_16x16x32_bf16 v[228:231], a[100:103], v[108:111], 0\n\t"
      "s_nop 5\n\t"
      "v_accvgpr_read_b32 v48, a140\n\t"
      "v_cmp_ngt_f32 vcc, s63, v48\n\t"
      "v_mfma_f32_16x16x32_bf16 v[232:235], a[104:107], v[108:111], 0\n\t"
      "s_or_b64 s[72:73], s[72:73], vcc\n\t"
      "v_rcp_f32 v48, v48\n\t"
      "v_mfma_f32_16x16x32_bf16 v[236:239], a[108:111], v[108:111], 0\n\t"
      "v_mov_b32 v49, v48\n\t"
      "v_pk_mul_f32 v[192:193], v[192:193], v[48:49]\n\t"
      "v_mfma_f32_16x16x32_bf16 v[240:243], a[112:115], v[108:111], 0\n\t"
      "v_pk_mul_f32 v[194:195], v[194:195], v[48:49]\n\t"
      "v_cvt_pk_bf16_f32 v132, v192, v193\n\t"
      "v_mfma_f32_16x16x32_bf16 v[244:247], a[116:119], v[108:111], 0\n\t"
      "v_cvt_pk_bf16_f32 v133, v194, v195\n\t"
      "v_pk_mul_f32 v[196:197], v[196:197], v[48:49]\n\t"
      "v_mfma_f32_16x16x32_bf16 v[248:251], a[120:123], v[108:111], 0\n\t"
      "ds_write_b64 v112, v[132:133] offset:8192\n\t"
      "v_mfma_f32_16x16x32_bf16 v[252:255], a[124:127], v[108:111], 0\n\t"
      "v_pk_mul_f32 v[198:199], v[198:199], v[48:49]\n\t"
      "v_cvt_pk_bf16_f32 v134, v196, v197\n\t"
      "v_mfma_f32_16x16x32_bf16 a[144:147], a[128:131], v[108:111], 0\n\t"
      "v_cvt_pk_bf16_f32 v135, v198, v199\n\t"
      "ds_write_b64 v113, v[134:135] offset:8192\n\t"
      "v_pk_mul_f32 v[200:201], v[200:201], v[48:49]\n\t"
      "v_pk_mul_f32 v[202:203], v[202:203], v[48:49]\n\t"
      "v_cvt_pk_bf16_f32 v140, v200, v201\n\t"
      "v_cvt_pk_bf16_f32 v141, v202, v203\n\t"
      "ds_write_b64 v114, v[140:141] offset:8192\n\t"
      "v_pk_mul_f32 v[204:205], v[204:205], v[48:49]\n\t"
      "v_pk_mul_f32 v[206:207], v[206:207], v[48:49]\n\t"
      "v_cvt_pk_bf16_f32 v142, v204, v205\n\t"
      "v_cvt_pk_bf16_f32 v143, v206, v207\n\t"
      "ds_write_b64 v115, v[142:143] offset:8192\n\t"
      "v_pk_mul_f32 v[208:209], v[208:209], v[48:49]\n\t"
      "v_pk_mul_f32 v[210:211], v[210:211], v[48:49]\n\t"
      "v_cvt_pk_bf16_f32 v148, v208, v209\n\t"
      "v_cvt_pk_bf16_f32 v149, v210, v211\n\t"
      "ds_write_b64 v116, v[148:149] offset:8192\n\t"
      "v_pk_mul_f32 v[212:213], v[212:213], v[48:49]\n\t"
      "v_pk_mul_f32 v[214:215], v[214:215], v[48:49]\n\t"
      "v_cvt_pk_bf16_f32 v150, v212, v213\n\t"
      "v_cvt_pk_bf16_f32 v151, v214, v215\n\t"
      "ds_write_b64 v117, v[150:151] offset:8192\n\t"
      "v_pk_mul_f32 v[216:217], v[216:217], v[48:49]\n\t"
      "v_pk_mul_f32 v[218:219], v[218:219], v[48:49]\n\t"
      "v_cvt_pk_bf16_f32 v156, v216, v217\n\t"
      "v_cvt_pk_bf16_f32 v157, v218, v219\n\t"
      "ds_write_b64 v118, v[156:157] offset:8192\n\t"
      "v_pk_mul_f32 v[220:221], v[220:221], v[48:49]\n\t"
      "v_pk_mul_f32 v[222:223], v[222:223], v[48:49]\n\t"
      "v_cvt_pk_bf16_f32 v158, v220, v221\n\t"
      "v_cvt_pk_bf16_f32 v159, v222, v223\n\t"
      "ds_write_b64 v119, v[158:159] offset:8192\n\t"
      "ds_read_b128 v[132:135], v120 offset:8192\n\t"
      "v_accvgpr_read_b32 v50, a144\n\t"
      "ds_read_b128 v[140:143], v121 offset:8192\n\t"
      "v_cmp_ngt_f32 vcc, s63, v50\n\t"
      "ds_read_b128 v[148:151], v122 offset:8192\n\t"
      "s_or_b64 s[72:73], s[72:73], vcc\n\t"
      "v_rcp_f32 v50, v50\n\t"
      "ds_read_b128 v[156:159], v123 offset:8192\n\t"
      "v_mov_b32 v51, v50\n\t"
      "v_pk_mul_f32 v[224:225], v[224:225], v[50:51]\n\t"
      "v_pk_mul_f32 v[226:227], v[226:227], v[50:51]\n\t"
      "v_cvt_pk_bf16_f32 v164, v224, v225\n\t"
      "v_cvt_pk_bf16_f32 v165, v226, v227\n\t"
      "ds_write_b64 v112, v[164:165] offset:12288\n\t"
      "v_pk_mul_f32 v[228:229], v[228:229], v[50:51]\n\t"
      "v_pk_mul_f32 v[230:231], v[230:231], v[50:51]\n\t"
      "v_cvt_pk_bf16_f32 v166, v228, v229\n\t"
      "v_cvt_pk_bf16_f32 v167, v230, v231\n\t"
      "ds_write_b64 v113, v[166:167] offset:12288\n\t"
      "v_pk_mul_f32 v[232:233], v[232:233], v[50:51]\n\t"
      "v_pk_mul_f32 v[234:235], v[234:235], v[50:51]\n\t"
      "v_cvt_pk_bf16_f32 v172, v232, v233\n\t"
      "v_cvt_pk_bf16_f32 v173, v234, v235\n\t"
      "ds_write_b64 v114, v[172:173] offset:12288\n\t"
      "v_pk_mul_f32 v[236:237], v[236:237], v[50:51]\n\t"
      "v_pk_mul_f32 v[238:239], v[238:239], v[50:51]\n\t"
      "v_cvt_pk_bf16_f32 v174, v236, v237\n\t"
      "v_cvt_pk_bf16_f32 v175, v238, v239\n\t"
      "ds_write_b64 v115, v[174:175] offset:12288\n\t"
      "v_pk_mul_f32 v[240:241], v[240:241], v[50:51]\n\t"
      "v_pk_mul_f32 v[242:243], v[242:243], v[50:51]\n\t"
      "v_cvt_pk_bf16_f32 v180, v240, v241\n\t"
      "v_cvt_pk_bf16_f32 v181, v242, v243\n\t"
      "ds_write_b64 v116, v[180:181] offset:12288\n\t"
      "v_pk_mul_f32 v[244:245], v[244:245], v[50:51]\n\t"
      "v_pk_mul_f32 v[246:247], v[246:247], v[50:51]\n\t"
      "v_cvt_pk_bf16_f32 v182, v244, v245\n\t"
      "v_cvt_pk_bf16_f32 v183, v246, v247\n\t"
      "ds_write_b64 v117, v[182:183] offset:12288\n\t"
      "v_pk_mul_f32 v[248:249], v[248:249], v[50:51]\n\t"
      "v_pk_mul_f32 v[250:251], v[250:251], v[50:51]\n\t"
      "v_cvt_pk_bf16_f32 v188, v248, v249\n\t"
      "v_cvt_pk_bf16_f32 v189, v250, v251\n\t"
      "ds_write_b64 v118, v[188:189] offset:12288\n\t"
      "v_pk_mul_f32 v[252:253], v[252:253], v[50:51]\n\t"
      "v_pk_mul_f32 v[254:255], v[254:255], v[50:51]\n\t"
      "v_cvt_pk_bf16_f32 v190, v252, v253\n\t"
      "v_cvt_pk_bf16_f32 v191, v254, v255\n\t"
      "ds_write_b64 v119, v[190:191] offset:12288\n\t"
      "ds_read_b128 v[164:167], v120 offset:12288\n\t"
      "ds_read_b128 v[172:175], v121 offset:12288\n\t"
      "ds_read_b128 v[180:183], v122 offset:12288\n\t"
      "ds_read_b128 v[188:191], v123 offset:12288\n\t"
      "s_cmp_lg_u64 s[72:73], 0\n\t"
      "s_cselect_b32 s76, 1, 0\n\t"
      "s_mov_b32 s77, 0\n\t"
      "s_lshl_b64 s[76:77], s[76:77], s59\n\t"
      "s_or_b64 s[74:75], s[74:75], s[76:77]\n\t"
      "s_branch Lnext%=\n\t"
      "Lend%=:\n\t"
      "s_add_u32 s70, s61, 0x10000\n\t"
      "s_waitcnt lgkmcnt(8)\n\t"
      "buffer_store_dwordx4 v[132:135], v53, s[48:51], s61 offen sc1 nt\n\t"
      "buffer_store_dwordx4 v[140:143], v54, s[48:51], s61 offen sc1 nt\n\t"
      "buffer_store_dwordx4 v[148:151], v55, s[48:51], s61 offen sc1 nt\n\t"
      "buffer_store_dwordx4 v[156:159], v56, s[48:51], s61 offen sc1 nt\n\t"
      "s_waitcnt lgkmcnt(3)\n\t"
      "buffer_store_dwordx4 v[164:167], v53, s[48:51], s70 offen sc1 nt\n\t"
      "s_waitcnt lgkmcnt(2)\n\t"
      "buffer_store_dwordx4 v[172:175], v54, s[48:51], s70 offen sc1 nt\n\t"
      "s_waitcnt lgkmcnt(1)\n\t"
      "buffer_store_dwordx4 v[180:183], v55, s[48:51], s70 offen sc1 nt\n\t"
      "s_waitcnt lgkmcnt(0)\n\t"
      "buffer_store_dwordx4 v[188:191], v56, s[48:51], s70 offen sc1 nt\n\t"
      "s_mov_b32 s61, s78\n\t"
      "s_mov_b32 s50, s82\n\t"
      "s_waitcnt vmcnt(0) lgkmcnt(0)\n\t"
      "s_mov_b64 %[fail], s[74:75]\n\t"
      "s_cmp_lg_u64 s[74:75], 0\n\t"
      "s_cbranch_scc1 Lfin%=\n\t"
      "s_endpgm\n\t"
      "Lpsub%=:\n\t"
      "s_cmp_lt_i32 s87, 0\n\t"
      "s_cbranch_scc1 Lpnx%=\n\t"
      "s_mov_b32 s55, s88\n\t"
      "s_mov_b32 s56, s87\n\t"
      "s_mov_b32 s87, -1\n\t"
      "s_mov_b32 s86, 32768\n\t"
      "s_setpc_b64 s[84:85]\n\t"
      "Lpnx%=:\n\t"
      "s_add_u32 s57, s57, 1\n\t"
      "v_readlane_b32 s55, %[ta], s57\n\t"
      "v_readlane_b32 s38, %[tb], s57\n\t"
      "v_readlane_b32 s71, %[tc], s57\n\t"
      "s_mov_b32 s86, -32768\n\t"
      "s_nop 0\n\t"
      "s_add_u32 s42, s38, 4096\n\t"
      "s_and_b32 s56, s71, 1\n\t"
      "s_lshr_b32 s71, s71, 1\n\t"
      "s_sub_u32 s87, s71, s56\n\t"
      "s_sub_u32 s87, s87, 1\n\t"
      "s_lshl_b32 s71, s71, 15\n\t"
      "s_sub_u32 s88, s55, s71\n\t"
      "s_setpc_b64 s[84:85]\n\t"
      "Lps2_%=:\n\t"
      "s_call_b64 s[84:85], Lpsub%=\n\t"
      "s_branch Lpr2_%=\n\t"
      "Lqs3_%=:\n\t"
      "s_call_b64 s[84:85], Lqsub%=\n\t"
      "s_branch Lqr3_%=\n\t"
      "Lps5_%=:\n\t"
      "s_call_b64 s[84:85], Lpsub%=\n\t"
      "s_branch Lpr5_%=\n\t"
      "Lps7_%=:\n\t"
      "s_call_b64 s[84:85], Lpsub%=\n\t"
      "s_branch Lpr7_%=\n\t"
      "Lps9_%=:\n\t"
      "s_call_b64 s[84:85], Lpsub%=\n\t"
      "s_branch Lpr9_%=\n\t"
      "Lps11_%=:\n\t"
      "s_call_b64 s[84:85], Lpsub%=\n\t"
      "s_branch Lpr11_%=\n\t"
      "Lqs12_%=:\n\t"
      "s_call_b64 s[84:85], Lqsub%=\n\t"
      "s_branch Lqr12_%=\n\t"
      "Lps14_%=:\n\t"
      "s_call_b64 s[84:85], Lpsub%=\n\t"
      "s_branch Lpr14_%=\n\t"
      "Lps16_%=:\n\t"
      "s_call_b64 s[84:85], Lpsub%=\n\t"
      "s_branch Lpr16_%=\n\t"
      "Lps18_%=:\n\t"
      "s_call_b64 s[84:85], Lpsub%=\n\t"
      "s_branch Lpr18_%=\n\t"
      "Lqsub%=:\n\t"
      "s_waitcnt lgkmcnt(0)\n\t"
      "s_add_u32 m0, s91, 0\n\t"
      "s_nop 0\n\t"
      "buffer_load_dwordx4 %[vqa], s[44:47], s92 offen offset:0  lds\n\t"
      "s_add_u32 s92, s92, 15360\n\t"
      "buffer_load_dwordx4 %[vqa], s[44:47], s92 offen offset:1024  lds\n\t"
      "s_add_u32 s92, s92, 15360\n\t"
      "buffer_load_dwordx4 %[vqb], s[44:47], s92 offen offset:2048  lds\n\t"
      "s_add_u32 s92, s92, 15360\n\t"
      "buffer_load_dwordx4 %[vqb], s[44:47], s92 offen offset:3072  lds\n\t"
      "s_add_u32 s92, s92, 19456\n\t"
      "s_add_u32 m0, s91, 4096\n\t"
      "s_nop 0\n\t"
      "buffer_load_dwordx4 %[vqa], s[44:47], s92 offen offset:0  lds\n\t"
      "s_add_u32 s92, s92, 15360\n\t"
      "buffer_load_dwordx4 %[vqa], s[44:47], s92 offen offset:1024  lds\n\t"
      "s_add_u32 s92, s92, 15360\n\t"
      "buffer_load_dwordx4 %[vqb], s[44:47], s92 offen offset:2048  lds\n\t"
      "s_add_u32 s92, s92, 15360\n\t"
      "buffer_load_dwordx4 %[vqb], s[44:47], s92 offen offset:3072  lds\n\t"
      "s_sub_u32 s92, s92, 242688\n\t"
      "s_sub_u32 s91, s91, 8192\n\t"
      "s_sub_u32 s93, s93, 1\n\t"
      "s_cmp_lg_u32 s93, 0\n\t"
      "s_cbranch_scc1 Lqsub%=\n\t"
      "s_setpc_b64 s[84:85]\n\t"
      "Lps19_%=:\n\t"
      "s_call_b64 s[84:85], Lpsub%=\n\t"
      "s_branch Lpr19_%=\n\t"
      "Lps20_%=:\n\t"
      "s_call_b64 s[84:85], Lpsub%=\n\t"
      "s_branch Lpr20_%=\n\t"
      "Lps21_%=:\n\t"
      "s_call_b64 s[84:85], Lpsub%=\n\t"
      "s_branch Lpr21_%=\n\t"
      "Lps22_%=:\n\t"
      "s_call_b64 s[84:85], Lpsub%=\n\t"
      "s_branch Lpr22_%=\n\t"
      "Lps23_%=:\n\t"
      "s_call_b64 s[84:85], Lpsub%=\n\t"
      "s_branch Lpr23_%=\n\t"
      "Lps24_%=:\n\t"
      "s_call_b64 s[84:85], Lpsub%=\n\t"
      "s_branch Lpr24_%=\n\t"
      "Lfin%=:\n\t"
      : [fail] "=s"(fail)
      : [ta] "v"(ta), [tb] "v"(tb), [tc] "v"(tc), [vk0] "v"(vk0), [vk1] "v"(vk1), [vv0] "v"(vv0), [vv1] "v"(vv1), [kb] "v"(kb), [vb0] "v"(vb0), [vb1] "v"(vb1),
        [vqa] "v"(vqa), [vqb] "v"(vqb), [qb] "v"(qb), [early] "s"(__builtin_amdgcn_readfirstlane(early)), [vo] "v"(vo), [wa] "v"(wa), [ra] "v"(ra), [md] "v"(md), [klo] "s"((unsigned)kp), [khi] "s"((unsigned)(kp >> 32)), [vlo] "s"((unsigned)vp), [vhi] "s"((unsigned)(vp >> 32)),
        [qlo] "s"((unsigned)qp), [qhi] "s"((unsigned)(qp >> 32)), [olo] "s"((unsigned)op), [ohi] "s"((unsigned)(op >> 32)), [woff] "s"(w * 4096), [n] "s"(__builtin_amdgcn_readfirstlane(cnt)), [thr] "s"(THR_BITS)
      : "v32","v33","v34","v35","v36","v37","v38","v39","v40","v41","v42","v43","v44","v45","v46","v47","v48","v49","v50","v51","v52","v53","v54","v55","v56","v57","v58","v59","v60","v61","v62","v63","v64","v65","v66","v67","v68","v69","v70","v71","v72","v73","v74","v75","v76","v77","v78","v79","v80","v81","v82","v83","v84","v85","v86","v87","v88","v89","v90","v91","v92","v93","v94","v95","v96","v97","v98","v99","v100","v101","v102","v103","v104","v105","v106","v107","v108","v109","v110","v111","v112","v113","v114","v115","v116","v117","v118","v119","v120","v121","v122","v123","v124","v125","v126","v127","v128","v129","v130","v131","v132","v133","v134","v135","v136","v137","v138","v139","v140","v141","v142","v143","v144","v145","v146","v147","v148","v149","v150","v151","v152","v153","v154","v155","v156","v157","v158","v159","v160","v161","v162","v163","v164","v165","v166","v167","v168","v169","v170","v171","v172","v173","v174","v175","v176","v177","v178","v179","v180","v181","v182","v183","v184","v185","v186","v187","v188","v189","v190","v191","v192","v193","v194","v195","v196","v197","v198","v199","v200","v201","v202","v203","v204","v205","v206","v207","v208","v209","v210","v211","v212","v213","v214","v215","v216","v217","v218","v219","v220","v221","v222","v223","v224","v225","v226","v227","v228","v229","v230","v231","v232","v233","v234","v235","v236","v237","v238","v239","v240","v241","v242","v243","v244","v245","v246","v247","v248","v249","v250","v251","v252","v253","v254","v255","a0","a1","a2","a3","a4","a5","a6","a7","a8","a9","a10","a11","a12","a13","a14","a15","a16","a17","a18","a19","a20","a21","a22","a23","a24","a25","a26","a27","a28","a29","a30","a31","a32","a33","a34","a35","a36","a37","a38","a39","a40","a41","a42","a43","a44","a45","a46","a47","a48","a49","a50","a51","a52","a53","a54","a55","a56","a57","a58","a59","a60","a61","a62","a63","a64","a65","a66","a67","a68","a69","a70","a71","a72","a73","a74","a75","a76","a77","a78","a79","a80","a81","a82","a83","a84","a85","a86","a87","a88","a89","a90","a91","a92","a93","a94","a95","a96","a97","a98","a99","a100","a101","a102","a103","a104","a105","a106","a107","a108","a109","a110","a111","a112","a113","a114","a115","a116","a117","a118","a119","a120","a121","a122","a123","a124","a125","a126","a127","a128","a129","a130","a131","a132","a133","a134","a135","a136","a137","a138","a139","a140","a141","a142","a143","a144","a145","a146","a147","a148","a149","a150","a151","a152","a153","a154","a155","a156","a157","a158","a159","a160","a161","a162","a163","a164","a165","a166","a167","a168","a169","a170","a171","a172","a173","a174","a175","a176","a177","a178","a179","a180","a181","a182","a183","a184","a185","a186","a187","a188","a189","a190","a191","a192","a193","a194","a195","a196","a197","a198","a199","a200","a201","a202","a203","a204","a205","a206","a207","a208","a209","a210","a211","a212","a213","a214","a215","a216","a217","a218","a219","a220","a221","a222","a223","a224","a225","a226","a227","a228","a229","a230","a231","a232","a233","a234","a235","a236","a237","a238","a239","a240","a241","a242","a243","a244","a245","a246","a247","a248","a249","a250","a251","a252","a253","a254","a255","s36","s37","s38","s39","s40","s41","s42","s43","s44","s45","s46","s47","s48","s49","s50","s51","s52","s53","s54","s55","s56","s57","s58","s59","s60","s61","s62","s63","s64","s65","s66","s67","s68","s69","s70","s71","s72","s73","s74","s75","s76","s77","s78","s79","s80","s81","s82","s83","s84","s85","s86","s87","s88","s89","s90","s91","s92","s93","s94","s95","vcc","scc","memory");
    while (fail) {
        const int it = __builtin_ctzll(fail); fail &= fail - 1;
        const int s0 = rl(s0m, it), L = rl(Lm, it), c = rl(tc, it);
        for (int e = 0; e <= (c & 1); ++e) slow_rows(q, k, v, out, s0, L, g * 4 + w, ((c >> 1) - e) * 32);
    }
}

extern "C" void kernel_launch(void* const* d_in, const int* in_sizes, int n_in, void* d_out, int out_size, void* d_ws, size_t ws_size,
                              hipStream_t stream) {
    const int nseq = in_sizes[3] - 1, T = in_sizes[0] / (HQ * D);
    const u16 *q = (const u16*)d_in[0], *k = (const u16*)d_in[1], *v = (const u16*)d_in[2];
    if (nseq >= 1 && nseq <= 64 && T <= 30000) {
        (void)hipFuncSetAttribute((const void*)attn_kernel, hipFuncAttributeMaxDynamicSharedMemorySize, 160 * 1024);
        attn_kernel<<<256, 256, 160 * 1024, stream>>>(q, k, v, (const int*)d_in[3], nseq, T, (u16*)d_out
            );
    } else {
        slow_kernel<<<1024, 256, 0, stream>>>(q, k, v, (const int*)d_in[3], nseq, (u16*)d_out);
    }
}
```
